```python
import torch

def _softmax(x, axis=-1):
    return torch.softmax(x, dim=axis)
def _gelu(x):
    return torch.nn.functional.gelu(x, approximate="tanh")
def _silu(x):
    return torch.nn.functional.silu(x)
def _sigmoid(x):
    return torch.sigmoid(x)
def _layernorm(x, axis=-1, eps=1e-5):
    m = x.mean(dim=axis, keepdim=True)
    v = x.var(dim=axis, keepdim=True, unbiased=False)
    return (x - m) / torch.sqrt(v + eps)
def _scatter_add(tgt, idx, src, axis=0):
    return tgt.clone().index_add_(axis, idx, src)
def _segment_sum(src, seg, n):
    out = torch.zeros((n,) + src.shape[1:], dtype=src.dtype)
    return out.index_add_(0, seg, src)
def _pad(x, pw):
    flat = tuple(v for pair in reversed(pw) for v in pair)
    return torch.nn.functional.pad(x, flat)

kernel_name = 'standalone__mha_block_cached__near_ridge'

def setup_inputs():

    _w = (lambda a: (lambda t: t.to(torch.bfloat16) if t.is_floating_point() else t)(torch.as_tensor(a)))
    B, Sq, D = 8, 144, 576
    H, Sk, Dh = 32, 512, 128
    return {
        "x": _w((lambda *s: torch.randn(*s, dtype=torch.bfloat16) * 0.8)(B, Sq, D)),
        "k_cache": _w((lambda *s: torch.randn(*s, dtype=torch.bfloat16) * 0.8)(B, H, Sk, Dh)),
        "v_cache": _w((lambda *s: torch.randn(*s, dtype=torch.bfloat16) * 0.05)(B, H, Sk, Dh)),
        "wq": _w((lambda *s: torch.randn(*s, dtype=torch.bfloat16) * 0.8)(D, H * Dh) * (D ** -0.5)),
        "wo": _w((lambda *s: torch.randn(*s, dtype=torch.bfloat16) * 0.05)(H * Dh, D) * ((H * Dh) ** -0.5)),
    }

def reference(x, k_cache, v_cache, wq, wo):
    B, Sq, D = x.shape
    H, Dh = 32, 128
    q = (lambda x, *d: x.permute(*d))((x @ wq).reshape(B, Sq, H, Dh), 0, 2, 1, 3)
    scores = torch.einsum("bhqd,bhkd->bhqk", q, k_cache) * (Dh ** -0.5)
    p = _softmax(scores, axis=-1)
    o = torch.einsum("bhqk,bhkd->bhqd", p, v_cache)
    return (lambda x, *d: x.permute(*d))(o, 0, 2, 1, 3).reshape(B, Sq, H * Dh) @ wo
```

```cpp
#include <hip/hip_runtime.h>
#include <hip/hip_bf16.h>
constexpr int B=8,SQ=144,D=576,H=32,SK=512,DH=128;
typedef __hip_bfloat16 bf16;
__global__ void k_qproj(const bf16* x,const bf16* wq,bf16* q){
  int n=blockIdx.x*256+threadIdx.x; int row=blockIdx.y;
  float acc=0;
  for(int k=0;k<D;k++) acc+=__bfloat162float(x[row*D+k])*__bfloat162float(wq[(size_t)k*4096+n]);
  q[(size_t)row*4096+n]=__float2bfloat16(acc);
}
__global__ void k_attn(const bf16* q,const bf16* kc,const bf16* vc,bf16* o){
  int bh=blockIdx.y; int b=bh/H,h=bh%H; int r=blockIdx.x;
  __shared__ float qs[DH]; __shared__ float s[SK]; __shared__ float red[128];
  int t=threadIdx.x;
  qs[t]=__bfloat162float(q[(size_t)(b*SQ+r)*4096+h*DH+t]);
  __syncthreads();
  const bf16* K=kc+(size_t)bh*SK*DH; const bf16* V=vc+(size_t)bh*SK*DH;
  float mx=-1e30f;
  for(int j=t;j<SK;j+=128){ float a=0; for(int d=0;d<DH;d++) a+=qs[d]*__bfloat162float(K[j*DH+d]); a*=0.08838834764831845f; s[j]=a; mx=fmaxf(mx,a);}
  red[t]=mx; __syncthreads();
  for(int o2=64;o2>0;o2>>=1){ if(t<o2) red[t]=fmaxf(red[t],red[t+o2]); __syncthreads(); }
  mx=red[0]; __syncthreads();
  float sum=0; for(int j=t;j<SK;j+=128){ float e=__expf(s[j]-mx); s[j]=e; sum+=e; }
  red[t]=sum; __syncthreads();
  for(int o2=64;o2>0;o2>>=1){ if(t<o2) red[t]+=red[t+o2]; __syncthreads(); }
  sum=red[0];
  float a=0; for(int j=0;j<SK;j++) a+=s[j]*__bfloat162float(V[j*DH+t]);
  o[(size_t)(b*SQ+r)*4096+h*DH+t]=__float2bfloat16(a/sum);
}
__global__ void k_oproj(const bf16* o,const bf16* wo,bf16* out){
  int n=blockIdx.x*64+threadIdx.x; int row=blockIdx.y;
  float acc=0;
  for(int k=0;k<4096;k++) acc+=__bfloat162float(o[(size_t)row*4096+k])*__bfloat162float(wo[(size_t)k*D+n]);
  out[row*D+n]=__float2bfloat16(acc);
}
extern "C" void kernel_launch(void* const* d_in, const int* in_sizes, int n_in,
                              void* d_out, int out_size, void* d_ws, size_t ws_size,
                              hipStream_t stream) {
  const bf16* x=(const bf16*)d_in[0]; const bf16* kc=(const bf16*)d_in[1]; const bf16* vc=(const bf16*)d_in[2];
  const bf16* wq=(const bf16*)d_in[3]; const bf16* wo=(const bf16*)d_in[4];
  bf16* q=(bf16*)d_ws; bf16* o=q+(size_t)1152*4096;
  k_qproj<<<dim3(16,1152),256,0,stream>>>(x,wq,q);
  k_attn<<<dim3(SQ,B*H),128,0,stream>>>(q,kc,vc,o);
  k_oproj<<<dim3(9,1152),64,0,stream>>>(o,wo,(bf16*)d_out);
}
```

```cpp
#include <hip/hip_runtime.h>
#include <hip/hip_bf16.h>
#include <utility>

namespace {

constexpr int NB = 8, SQ = 144, DM = 576, NH = 32, SK = 512, DH = 128;

typedef __attribute__((ext_vector_type(4))) short s16x4;
typedef __attribute__((ext_vector_type(8))) short s16x8;
typedef __attribute__((ext_vector_type(4))) float f32x4;
typedef __attribute__((ext_vector_type(16))) float f32x16;
typedef __attribute__((ext_vector_type(8))) __bf16 bf16x8;
typedef __attribute__((ext_vector_type(2))) __bf16 bf16x2;
typedef __attribute__((ext_vector_type(2))) float f32x2;
typedef __attribute__((ext_vector_type(4))) unsigned u32x4;
typedef __attribute__((ext_vector_type(2))) unsigned u32x2;
#define LDSP __attribute__((address_space(3)))
#define DI __device__ __forceinline__

DI f32x4 mfma16(s16x8 a, s16x8 b, f32x4 c) {
  return __builtin_amdgcn_mfma_f32_16x16x32_bf16(__builtin_bit_cast(bf16x8, a), __builtin_bit_cast(bf16x8, b), c, 0, 0, 0);
}
DI f32x4 mfma16k16(s16x4 a, s16x4 b, f32x4 c) { return __builtin_amdgcn_mfma_f32_16x16x16bf16_1k(a, b, c, 0, 0, 0); }
DI f32x16 mfma32(s16x8 a, s16x8 b, f32x16 c) {
  return __builtin_amdgcn_mfma_f32_32x32x16_bf16(__builtin_bit_cast(bf16x8, a), __builtin_bit_cast(bf16x8, b), c, 0, 0, 0);
}
DI unsigned pk2(float lo, float hi) {
  f32x2 v = {lo, hi};
  bf16x2 r = __builtin_convertvector(v, bf16x2);
  return __builtin_bit_cast(unsigned, r);
}
DI s16x4 trread(unsigned addr) { return __builtin_amdgcn_ds_read_tr16_b64_v4i16((LDSP s16x4*)(size_t)addr); }
DI s16x8 ldsread16(unsigned addr) { return *(LDSP s16x8*)(size_t)addr; }
DI s16x8 cat(s16x4 a, s16x4 b) { return __builtin_shufflevector(a, b, 0, 1, 2, 3, 4, 5, 6, 7); }
DI float max3(float a, float b, float c) { return __builtin_fmaxf(__builtin_fmaxf(a, b), c); }
DI float xmax32(float v) {
  auto r = __builtin_amdgcn_permlane32_swap(__float_as_uint(v), __float_as_uint(v), false, false);
  return fmaxf(__uint_as_float(r[0]), __uint_as_float(r[1]));
}
DI float xmax16(float v) {
  auto r = __builtin_amdgcn_permlane16_swap(__float_as_uint(v), __float_as_uint(v), false, false);
  return fmaxf(__uint_as_float(r[0]), __uint_as_float(r[1]));
}
DI float xadd32(float v) {
  auto r = __builtin_amdgcn_permlane32_swap(__float_as_uint(v), __float_as_uint(v), false, false);
  return __uint_as_float(r[0]) + __uint_as_float(r[1]);
}
DI float xadd16(float v) {
  auto r = __builtin_amdgcn_permlane16_swap(__float_as_uint(v), __float_as_uint(v), false, false);
  return __uint_as_float(r[0]) + __uint_as_float(r[1]);
}

template <bool NT>
DI void dma16(const char* sbase, unsigned voff, unsigned lds) {
  if constexpr (NT)
    asm volatile("s_mov_b32 m0, %1\n\ts_nop 0\n\tglobal_load_lds_dwordx4 %0, %2 nt" ::"v"(voff), "s"(lds), "s"(sbase) : "memory");
  else
    asm volatile("s_mov_b32 m0, %1\n\ts_nop 0\n\tglobal_load_lds_dwordx4 %0, %2" ::"v"(voff), "s"(lds), "s"(sbase) : "memory");
}
DI void dma16_lo(const char* sbase, unsigned voff, unsigned lds) {
  asm volatile("s_mov_b32 m0, %1\n\ts_mov_b32 exec_hi, 0\n\tglobal_load_lds_dwordx4 %0, %2\n\ts_mov_b32 exec_hi, -1" ::"v"(voff), "s"(lds), "s"(sbase) : "memory");
}
template <bool NT, int IMM>
DI void dma16i(const char* sbase, unsigned voff, unsigned lds) {
  if constexpr (NT)
    asm volatile("s_add_u32 m0, %1, %3\n\ts_nop 0\n\tglobal_load_lds_dwordx4 %0, %2 nt" ::"v"(voff), "s"(lds), "s"(sbase), "i"(IMM) : "memory", "scc");
  else
    asm volatile("s_add_u32 m0, %1, %3\n\ts_nop 0\n\tglobal_load_lds_dwordx4 %0, %2" ::"v"(voff), "s"(lds), "s"(sbase), "i"(IMM) : "memory", "scc");
}
template <int IMM>
DI void dma16i_lo(const char* sbase, unsigned voff, unsigned lds) {
  asm volatile("s_add_u32 m0, %1, %3\n\ts_mov_b32 exec_hi, 0\n\tglobal_load_lds_dwordx4 %0, %2\n\ts_mov_b32 exec_hi, -1" ::"v"(voff), "s"(lds), "s"(sbase), "i"(IMM) : "memory", "scc");
}
template <int N> DI void wait_vm() { asm volatile("s_waitcnt vmcnt(%0)" ::"i"(N) : "memory"); }
DI void wait_vm_tiles(int n) {
  if (n <= 0) wait_vm<0>();
  else if (n == 1) wait_vm<4>();
  else if (n == 2) wait_vm<8>();
  else if (n == 3) wait_vm<12>();
  else if (n == 4) wait_vm<16>();
  else if (n == 5) wait_vm<20>();
  else if (n == 6) wait_vm<24>();
  else wait_vm<28>();
}
#define STAMP(i)
DI void barrier() { asm volatile("s_waitcnt lgkmcnt(0)\n\ts_barrier" ::: "memory"); }
template <int... I, class F> DI void for_seq(std::integer_sequence<int, I...>, F&& f) { (f(std::integral_constant<int, I>{}), ...); }
#define IC(n) std::integral_constant<int, n>{}
#define SGB(mask, n) __builtin_amdgcn_sched_group_barrier(mask, n, 0)
#define SBAR() __builtin_amdgcn_sched_barrier(0)

#define KV_NT true
#define LOOKAHEAD 4
#define HPRIO 2
#define EARLY_B 0

constexpr int SLOT = 17408;
constexpr int GRP = 1088;
constexpr int QPOS0 = 3 * SLOT;
constexpr int QX_OFF = 9 * SLOT;
constexpr int ST_OFF = QX_OFF + 4096;
constexpr int K1_LDS = ST_OFF + 512;
constexpr int CMB_OFF = 7 * SLOT;
constexpr float CSC = 0.08838834764831845f * 1.4426950408889634f;
constexpr float THR = 8.0f;

__global__ __launch_bounds__(512) void k1_attn(const char* __restrict__ x, const char* __restrict__ kc, const char* __restrict__ vc,
                                               const char* __restrict__ wq, char* __restrict__ ows) {
  __shared__ __attribute__((aligned(256))) char smem[K1_LDS];
  const int tid = threadIdx.x;
  const int lane = tid & 63;
  const int wid = __builtin_amdgcn_readfirstlane(tid >> 6);
  const int xcd = blockIdx.x & 7, idx = blockIdx.x >> 3;
  const int b = 4 * (xcd & 1) + (idx & 3);
  const int h = 8 * (xcd >> 1) + (idx >> 2);
  const unsigned lds0 = (unsigned)(size_t)(LDSP char*)smem;
  STAMP(0);

  if (wid >= 4) {
    const int hi = wid - 4;
    const char* xg = x + (size_t)(b * SQ) * (DM * 2);
    const char* wg = wq + h * 256;
    const char* kg = kc + (size_t)(b * NH + h) * (SK * DH * 2);
    const char* vg = vc + (size_t)(b * NH + h) * (SK * DH * 2);
    unsigned xoff[4], xoffe, woff[4], koff[4], voff[4];
    {
      const int cc = lane & 7, c16 = lane & 15;
#pragma unroll
      for (int n = 0; n < 4; ++n) {
        int rx = hi * 32 + n * 8 + (lane >> 3);
        xoff[n] = rx * (DM * 2) + ((cc ^ ((rx >> 1) & 7)) << 4);
        int r = hi * 16 + n * 4 + (lane >> 4);
        woff[n] = r * 8192 + ((c16 ^ (((r & 3) << 2) | (((r >> 3) & 1) << 1))) << 4);
        koff[n] = r * 256 + ((c16 ^ (r & 3)) << 4);
        voff[n] = r * 256 + (c16 << 4);
      }
      int rx = 128 + hi * 4 + ((lane >> 3) & 3);
      xoffe = rx * (DM * 2) + ((cc ^ ((rx >> 1) & 7)) << 4);
    }
    const unsigned lq = lds0 + QPOS0 + hi * 4096;
    const unsigned lqe = lds0 + QPOS0 + 32768 + hi * 512;
    const unsigned lt = lds0 + hi * (4 * GRP);
    auto issue_q = [&](auto SC) {
      constexpr int S = decltype(SC)::value;
      constexpr int P = (S % 3) * (2 * SLOT);
      const char* ws = wg + (size_t)S * (64 * 8192);
      const char* xs = xg + S * 128;
      dma16i<false, P>(ws, woff[0], lq); dma16i<false, P + 1024>(ws, woff[1], lq);
      dma16i<false, P + 2048>(ws, woff[2], lq); dma16i<false, P + 3072>(ws, woff[3], lq);
      dma16i<false, P + 16384>(xs, xoff[0], lq); dma16i<false, P + 16384 + 1024>(xs, xoff[1], lq);
      dma16i<false, P + 16384 + 2048>(xs, xoff[2], lq); dma16i<false, P + 16384 + 3072>(xs, xoff[3], lq);
      dma16i_lo<P>(xs, xoffe, lqe);
    };
    auto issue_t = [&](auto AC) {
      constexpr int A = decltype(AC)::value;
      constexpr int L = (A % 9) * SLOT;
      constexpr bool isK = (A == 0) || ((A & 1) && A != 15);
      if constexpr (isK) {
        const char* sp = kg + ((A + 1) >> 1) * 16384;
        dma16i<KV_NT, L>(sp, koff[0], lt); dma16i<KV_NT, L + GRP>(sp, koff[1], lt);
        dma16i<KV_NT, L + 2 * GRP>(sp, koff[2], lt); dma16i<KV_NT, L + 3 * GRP>(sp, koff[3], lt);
      } else {
        const char* sp = vg + ((A == 15) ? 7 : ((A - 2) >> 1)) * 16384;
        dma16i<KV_NT, L>(sp, voff[0], lt); dma16i<KV_NT, L + GRP>(sp, voff[1], lt);
        dma16i<KV_NT, L + 2 * GRP>(sp, voff[2], lt); dma16i<KV_NT, L + 3 * GRP>(sp, voff[3], lt);
      }
    };
    issue_q(IC(0));
    issue_q(IC(1));

    const int r16 = lane & 15, g = lane >> 4, qp = (lane >> 2) & 3, p = lane & 3;
    unsigned wbase[2], xbase[2], kb, vb;
    {
      const int ew = (qp << 1) | (g & 1);
#pragma unroll
      for (int t = 0; t < 2; ++t) wbase[t] = lds0 + QPOS0 + (8 * g + qp) * 256 + 32 * ((2 * hi + t) ^ ew) + 8 * p;
      const int rx = 128 + r16;
#pragma unroll
      for (int s = 0; s < 2; ++s) xbase[s] = lds0 + QPOS0 + 16384 + rx * 128 + (((4 * s + g) ^ ((rx >> 1) & 7)) << 4);
      kb = lds0 + (4 * hi + (r16 & 3)) * GRP + (r16 >> 2) * 256 + ((g ^ (r16 >> 2)) << 4);
      vb = lds0 + (4 * hi + qp) * GRP + g * 256 + 8 * p;
    }
    __builtin_amdgcn_s_setprio(HPRIO);
    f32x4 qa[2] = {f32x4{0.f, 0.f, 0.f, 0.f}, f32x4{0.f, 0.f, 0.f, 0.f}};
    for_seq(std::make_integer_sequence<int, 9>{}, [&](auto SC) {
      constexpr int S = decltype(SC)::value;
      wait_vm<(S < 8) ? 9 : 8>();
      STAMP(1 + 2 * S);
      barrier();
      STAMP(2 + 2 * S);
      constexpr unsigned P = (S % 3) * (2 * SLOT);
      s16x8 xb[2], wf[2][2];
#pragma unroll
      for (int ks = 0; ks < 2; ++ks) {
        xb[ks] = ldsread16(xbase[ks] + P);
#pragma unroll
        for (int t = 0; t < 2; ++t) wf[ks][t] = cat(trread(wbase[t] + P + ks * 8192), trread(wbase[t] + P + ks * 8192 + 1024));
      }
      SBAR();
      if constexpr (S <= 6) issue_q(IC(S + 2));
      if constexpr (S == 7) { issue_t(IC(0)); issue_t(IC(1)); }
      if constexpr (S == 8) { issue_t(IC(2)); issue_t(IC(3)); }
      SBAR();
#pragma unroll
      for (int ks = 0; ks < 2; ++ks)
#pragma unroll
        for (int t = 0; t < 2; ++t) qa[t] = mfma16(wf[ks][t], xb[ks], qa[t]);
    });
    {
      qa[0] *= CSC; qa[1] *= CSC;
      unsigned a0 = pk2(qa[0][0], qa[0][1]), a1 = pk2(qa[0][2], qa[0][3]);
      unsigned b0 = pk2(qa[1][0], qa[1][1]), b1 = pk2(qa[1][2], qa[1][3]);
      auto r0 = __builtin_amdgcn_permlane32_swap(a0, b0, false, false);
      auto r1 = __builtin_amdgcn_permlane32_swap(a1, b1, false, false);
      auto t0 = __builtin_amdgcn_permlane16_swap(r0[0], r0[1], false, false);
      auto t1 = __builtin_amdgcn_permlane16_swap(r1[0], r1[1], false, false);
      u32x4 w = {t0[0], t1[0], t0[1], t1[1]};
      *(LDSP u32x4*)(size_t)(lds0 + QX_OFF + hi * 1024 + lane * 16) = w;
    }
    s16x8 qf[4];
    float m_run = -__builtin_inff(), l_run = 0.f, al_pend = 1.f;
    f32x4 oa[8];
#pragma unroll
    for (int T = 0; T < 8; ++T) oa[T] = f32x4{0.f, 0.f, 0.f, 0.f};
    s16x4 p_cur = {0, 0, 0, 0}, p_nxt = {0, 0, 0, 0};
    for_seq(std::make_integer_sequence<int, 16>{}, [&](auto AC) {
      constexpr int A = decltype(AC)::value;
      constexpr int LAST = (A - 1 + LOOKAHEAD > 15) ? 15 : ((A - 1 + LOOKAHEAD < 3) ? 3 : A - 1 + LOOKAHEAD);
      wait_vm<4 * (LAST - A)>();
      STAMP(19 + 2 * A);
      barrier();
      STAMP(20 + 2 * A);
      constexpr unsigned SL = (A % 9) * SLOT;
      constexpr bool isK = (A == 0) || ((A & 1) && A != 15);
      if constexpr (A == 0) {
#pragma unroll
        for (int s = 0; s < 4; ++s) qf[s] = ldsread16(lds0 + QX_OFF + s * 1024 + lane * 16);
      }
      s16x8 kf[4]; s16x4 vf[8];
      if constexpr (isK) {
#pragma unroll
        for (int ks = 0; ks < 4; ++ks) kf[ks] = ldsread16(kb + SL + ks * 64);
      } else {
#pragma unroll
        for (int T = 0; T < 8; ++T) vf[T] = trread(vb + SL + T * 32);
      }
      SBAR();
      for_seq(std::make_integer_sequence<int, 16>{}, [&](auto TC) {
        constexpr int TT = decltype(TC)::value;
        if constexpr (TT > LAST && TT <= A + LOOKAHEAD) issue_t(TC);
      });
      SBAR();
      if constexpr (isK) {
        f32x4 s = {0.f, 0.f, 0.f, 0.f};
#pragma unroll
        for (int ks = 0; ks < 4; ++ks) s = mfma16(kf[ks], qf[ks], s);
        float mx = fmaxf(fmaxf(s[0], s[1]), fmaxf(s[2], s[3]));
        mx = xmax32(xmax16(mx));
        float m_new = fmaxf(m_run, mx);
        float alpha = __builtin_amdgcn_exp2f(m_run - m_new);
        m_run = m_new;
        l_run *= alpha;
        al_pend = alpha;
#pragma unroll
        for (int r = 0; r < 4; ++r) { s[r] = __builtin_amdgcn_exp2f(s[r] - m_new); l_run += s[r]; }
        u32x2 w = {pk2(s[0], s[1]), pk2(s[2], s[3])};
        p_nxt = __builtin_bit_cast(s16x4, w);
        if constexpr (A == 0) p_cur = p_nxt;
      } else {
#pragma unroll
        for (int T = 0; T < 8; ++T) oa[T] = mfma16k16(vf[T], p_cur, oa[T]);
        if constexpr (A != 15) {
#pragma unroll
          for (int T = 0; T < 8; ++T) oa[T] *= al_pend;
        }
        p_cur = p_nxt;
      }
    });
    {
      float l = xadd32(xadd16(l_run));
      if (g == 0) {
        f32x2 st = {m_run, l};
        *(LDSP f32x2*)(size_t)(lds0 + ST_OFF + (hi * 16 + r16) * 8) = st;
      }
#pragma unroll
      for (int T = 0; T < 8; ++T) *(LDSP f32x4*)(size_t)(lds0 + CMB_OFF + (hi * 8 + T) * 1024 + lane * 16) = oa[T];
    }
    barrier();
    {
      f32x2 st[4];
#pragma unroll
      for (int k = 0; k < 4; ++k) st[k] = *(LDSP f32x2*)(size_t)(lds0 + ST_OFF + (k * 16 + r16) * 8);
      float m = fmaxf(fmaxf(st[0][0], st[1][0]), fmaxf(st[2][0], st[3][0]));
      float sc[4], l = 0.f;
#pragma unroll
      for (int k = 0; k < 4; ++k) { sc[k] = __builtin_amdgcn_exp2f(st[k][0] - m); l += sc[k] * st[k][1]; }
      float inv = 1.0f / l;
      const int T = 2 * hi + (g & 1);
      f32x4 lo = {0.f, 0.f, 0.f, 0.f}, hi4 = {0.f, 0.f, 0.f, 0.f};
#pragma unroll
      for (int k = 0; k < 4; ++k) {
        unsigned base = lds0 + CMB_OFF + (k * 8 + T) * 1024 + r16 * 16;
        f32x4 a = *(LDSP f32x4*)(size_t)(base + (g >> 1) * 256);
        f32x4 c2 = *(LDSP f32x4*)(size_t)(base + (2 + (g >> 1)) * 256);
        lo += a * sc[k];
        hi4 += c2 * sc[k];
      }
      u32x4 w = {pk2(lo[0] * inv, lo[1] * inv), pk2(lo[2] * inv, lo[3] * inv), pk2(hi4[0] * inv, hi4[1] * inv), pk2(hi4[2] * inv, hi4[3] * inv)};
      *(u32x4*)(ows + ((size_t)(b * 9 + 8) * 128 + h * 4 + hi) * 1024 + lane * 16) = w;
    }
  } else {
    const int r32 = lane & 31, hh = lane >> 5, cb = (lane >> 4) & 1, qp = (lane >> 2) & 3, p = lane & 3;
    f32x16 acc[4];
#pragma unroll
    for (int T = 0; T < 4; ++T)
#pragma unroll
      for (int r = 0; r < 16; ++r) acc[T][r] = 0.f;
    s16x8 FA[8], FB[8];

    {
      unsigned wad[4], xad[4];
#pragma unroll
      for (int T = 0; T < 4; ++T) {
        unsigned ch = ((4 * T + 2 * cb + (p >> 1)) ^ ((qp << 2) | (hh << 1)));
        wad[T] = lds0 + QPOS0 + (8 * hh + qp) * 256 + ch * 16 + 8 * (p & 1);
      }
      const int rx = wid * 32 + r32;
#pragma unroll
      for (int ks = 0; ks < 4; ++ks) xad[ks] = lds0 + QPOS0 + 16384 + rx * 128 + (((2 * ks + hh) ^ ((rx >> 1) & 7)) << 4);
      s16x8 xa[2], xb[2];
      auto qhalf = [&](s16x8 (&CUR)[8], s16x8 (&xc)[2], s16x8 (&NXT)[8], s16x8 (&xn)[2], auto HC, auto DOMM, auto DOLD) {
        constexpr int HALF = decltype(HC)::value;
        for_seq(std::make_integer_sequence<int, 8>{}, [&](auto IC_) {
          constexpr int I = decltype(IC_)::value;
          if constexpr (decltype(DOMM)::value) { acc[I & 3] = mfma32(CUR[I], xc[I >> 2], acc[I & 3]); SBAR(); }
          if constexpr (decltype(DOLD)::value) {
            if constexpr (I == 0) { xn[0] = ldsread16(xad[2 * HALF]); xn[1] = ldsread16(xad[2 * HALF + 1]); }
            if constexpr (I < 4) {
#pragma unroll
              for (int e = 0; e < 2; ++e) {
                constexpr int dummy = 0; (void)dummy;
                const int F = 2 * I + e, k = F >> 2, T = F & 3;
                NXT[F] = cat(trread(wad[T] + (2 * HALF + k) * 4096), trread(wad[T] + (2 * HALF + k) * 4096 + 1024));
              }
            }
          }
          SBAR();
        });
      };
      using T_ = std::true_type; using F_ = std::false_type;
      STAMP(1);
      barrier();
      STAMP(2);
      qhalf(FB, xb, FA, xa, IC(0), F_{}, T_{});
      int pos = 0;
#pragma unroll 1
      for (int S = 0; S < 8; ++S) {
        qhalf(FA, xa, FB, xb, IC(1), T_{}, T_{});
        const unsigned d = (pos == 2) ? (unsigned)(-4 * SLOT) : (unsigned)(2 * SLOT);
        pos = (pos == 2) ? 0 : pos + 1;
#pragma unroll
        for (int T = 0; T < 4; ++T) { wad[T] += d; xad[T] += d; }
        STAMP(3 + 2 * S);
        barrier();
        STAMP(4 + 2 * S);
        SBAR();
        qhalf(FB, xb, FA, xa, IC(0), T_{}, T_{});
      }
      qhalf(FA, xa, FB, xb, IC(1), T_{}, T_{});
      qhalf(FB, xb, FA, xa, IC(0), T_{}, F_{});
    }

    s16x8 qf[8];
#pragma unroll
    for (int T = 0; T < 4; ++T) {
      acc[T] *= CSC;
#pragma unroll
      for (int s = 0; s < 2; ++s) {
        unsigned a0 = pk2(acc[T][8 * s + 0], acc[T][8 * s + 1]), a1 = pk2(acc[T][8 * s + 2], acc[T][8 * s + 3]);
        unsigned b0 = pk2(acc[T][8 * s + 4], acc[T][8 * s + 5]), b1 = pk2(acc[T][8 * s + 6], acc[T][8 * s + 7]);
        auto r0 = __builtin_amdgcn_permlane32_swap(a0, b0, false, false);
        auto r1 = __builtin_amdgcn_permlane32_swap(a1, b1, false, false);
        u32x4 w = {r0[0], r1[0], r0[1], r1[1]};
        qf[2 * T + s] = __builtin_bit_cast(s16x8, w);
      }
    }
#pragma unroll
    for (int T = 0; T < 4; ++T)
#pragma unroll
      for (int r = 0; r < 16; ++r) acc[T][r] = 0.f;

    unsigned kad[2], vad;
    {
      const int a_ = r32 >> 3, h_ = (r32 >> 2) & 1, j_ = r32 & 3;
      const int grp = 4 * (a_ >> 1) + j_, rin = 2 * (a_ & 1) + h_;
#pragma unroll
      for (int e = 0; e < 2; ++e) kad[e] = lds0 + grp * GRP + rin * 256 + (((2 * e + hh) ^ rin) << 4);
      vad = lds0 + qp * GRP + hh * 256 + 32 * cb + 8 * p;
    }
    f32x16 s0, s1, negm;
    u32x4 pf[4];
    float l_run = 0.f, al_pend = 1.f, mx = 0.f, ps0 = 0.f, ps1 = 0.f;
    bool pend = false;
#define PIN(x) asm volatile("" : "+v"(x))
    auto kread = [&](int F, int t) { return ldsread16(kad[F & 1] + t * (8 * GRP) + (F >> 1) * 64); };
    auto vread = [&](int F, int hf) {
      const unsigned o = (8 * hf + 4 * (F >> 2)) * GRP + 64 * (F & 3);
      return cat(trread(vad + o), trread(vad + o + 512));
    };
    auto EXP = [&](f32x16& s, int r, float& ps) { float e = __builtin_amdgcn_exp2f(s[r]); PIN(e); s[r] = e; ps += e; PIN(ps); };
    auto CVT = [&](const f32x16& s, int i, u32x4& lo, u32x4& hi2) {
      unsigned w = pk2(s[2 * i], s[2 * i + 1]); PIN(w);
      if (i < 4) lo[i] = w; else hi2[i - 4] = w;
    };
    auto MAX2 = [&](const f32x16& s, int i) { mx = max3(mx, s[2 * i], s[2 * i + 1]); PIN(mx); };
    auto asv = [](const u32x4& v) { return __builtin_bit_cast(s16x8, v); };

    auto H1 = [&](auto FIN) {
      for_seq(std::make_integer_sequence<int, 8>{}, [&](auto IC_) {
        constexpr int I = decltype(IC_)::value;
        if constexpr (I == 0) s0 = mfma32(FA[0], qf[0], negm); else s0 = mfma32(FA[I], qf[I], s0);
        if constexpr (I < 4) { FB[2 * I] = kread(2 * I, 1); FB[2 * I + 1] = kread(2 * I + 1, 1); }
        if constexpr (decltype(FIN)::value) {
          if constexpr (I < 5) { EXP(s1, 6 + 2 * I, ps0); EXP(s1, 7 + 2 * I, ps1); }
          if constexpr (I >= 5) {
            constexpr int c0 = (I - 5) * 3;
            CVT(s1, c0, pf[2], pf[3]); CVT(s1, c0 + 1, pf[2], pf[3]);
            if constexpr (c0 + 2 < 8) CVT(s1, c0 + 2, pf[2], pf[3]);
          }
        }
        SBAR();
      });
    };
    auto H2 = [&](auto RDV) {
      for_seq(std::make_integer_sequence<int, 8>{}, [&](auto IC_) {
        constexpr int I = decltype(IC_)::value;
        if constexpr (I == 0) s1 = mfma32(FB[0], qf[0], negm); else s1 = mfma32(FB[I], qf[I], s1);
        if constexpr (decltype(RDV)::value && I < 4) { FA[2 * I] = vread(2 * I, 0); FA[2 * I + 1] = vread(2 * I + 1, 0); }
        if constexpr (I == 4) { mx = fmaxf(s0[0], s0[1]); PIN(mx); MAX2(s0, 1); }
        if constexpr (I > 4) { MAX2(s0, 2 * (I - 4)); MAX2(s0, 2 * (I - 4) + 1); }
        SBAR();
      });
    };
    auto rescale_o = [&]() {
      if (pend) {
#pragma unroll
        for (int T = 0; T < 4; ++T) acc[T] *= al_pend;
        pend = false;
      }
    };
    auto H3 = [&]() {
      for_seq(std::make_integer_sequence<int, 4>{}, [&](auto IC_) {
        constexpr int I = decltype(IC_)::value;
        acc[I & 3] = mfma32(FA[I], asv(pf[I >> 2]), acc[I & 3]);
        FB[2 * I] = vread(2 * I, 1); FB[2 * I + 1] = vread(2 * I + 1, 1);
        if constexpr (I >= 1) { MAX2(s1, 3 * (I - 1)); MAX2(s1, 3 * (I - 1) + 1); if constexpr (I < 3) MAX2(s1, 3 * (I - 1) + 2); }
        SBAR();
      });
      mx = xmax32(mx);
      if (__builtin_expect(__any(mx > THR), 0)) {
        float d = fmaxf(mx, 0.f);
        al_pend = __builtin_amdgcn_exp2f(-d);
        pend = true;
        l_run = (l_run + ps0 + ps1) * al_pend;
        ps0 = 0.f; ps1 = 0.f;
#pragma unroll
        for (int r = 0; r < 16; ++r) { negm[r] -= d; s0[r] -= d; s1[r] -= d; }
      }
      SBAR();
      for_seq(std::make_integer_sequence<int, 4>{}, [&](auto IC_) {
        constexpr int I = 4 + decltype(IC_)::value;
        acc[I & 3] = mfma32(FA[I], asv(pf[I >> 2]), acc[I & 3]);
        EXP(s0, 2 * (I - 4), ps0); EXP(s0, 2 * (I - 4) + 1, ps1);
        SBAR();
      });
    };
    auto H4 = [&](auto NEXTK) {
      for_seq(std::make_integer_sequence<int, 8>{}, [&](auto IC_) {
        constexpr int I = decltype(IC_)::value;
        acc[I & 3] = mfma32(FB[I], asv(pf[2 + (I >> 2)]), acc[I & 3]);
        if constexpr (I < 4) {
          if constexpr (decltype(NEXTK)::value) { FA[2 * I] = kread(2 * I, 0); FA[2 * I + 1] = kread(2 * I + 1, 0); }
          else { FA[2 * I] = vread(2 * I, 0); FA[2 * I + 1] = vread(2 * I + 1, 0); }
          EXP(s0, 8 + 2 * I, ps0); EXP(s0, 9 + 2 * I, ps1);
        } else {
          CVT(s0, 2 * (I - 4), pf[0], pf[1]); CVT(s0, 2 * (I - 4) + 1, pf[0], pf[1]);
          if constexpr (I < 7) { EXP(s1, 2 * (I - 4), ps0); EXP(s1, 2 * (I - 4) + 1, ps1); }
        }
        SBAR();
      });
    };
    using T_ = std::true_type; using F_ = std::false_type;
    auto adv = [&](unsigned& a, int& slot) {
      slot += 2;
      unsigned d = 2 * SLOT;
      if (slot >= 9) { slot -= 9; d = (unsigned)(-7 * SLOT); }
      a += d;
    };

#pragma unroll
    for (int r = 0; r < 16; ++r) negm[r] = 0.f;
    STAMP(19);
    barrier();
    STAMP(20);
#pragma unroll
    for (int F = 0; F < 8; ++F) FA[F] = kread(F, 0);
    SBAR();
    H1(F_{});
    H2(F_{});
#pragma unroll
    for (int i = 0; i < 8; ++i) MAX2(s1, i);
    mx = xmax32(mx);
#pragma unroll
    for (int r = 0; r < 16; ++r) { negm[r] = -mx; s0[r] -= mx; s1[r] -= mx; }
    int kslot = 1, vslot = 2, kslot2 = 1;
    kad[0] += SLOT; kad[1] += SLOT; vad += 2 * SLOT;
    STAMP(21);
    barrier();
    STAMP(22);
#pragma unroll
    for (int F = 0; F < 8; ++F) FA[F] = kread(F, 0);
#pragma unroll
    for (int r = 0; r < 16; r += 2) { EXP(s0, r, ps0); EXP(s0, r + 1, ps1); }
#pragma unroll
    for (int i = 0; i < 8; ++i) CVT(s0, i, pf[0], pf[1]);
#pragma unroll
    for (int r = 0; r < 6; r += 2) { EXP(s1, r, ps0); EXP(s1, r + 1, ps1); }
    SBAR();
#pragma unroll 1
    for (int j = 0; j < 6; ++j) {
      H1(T_{});
      rescale_o();
      STAMP(23 + 4 * j);
      barrier();
      STAMP(24 + 4 * j);
      SBAR();
      H2(T_{});
      H3();
      adv(kad[0], kslot); adv(kad[1], kslot2); adv(vad, vslot);
      STAMP(25 + 4 * j);
      barrier();
      STAMP(26 + 4 * j);
      SBAR();
      H4(T_{});
    }
    H1(T_{});
    rescale_o();
    STAMP(47);
    barrier();
    STAMP(48);
    SBAR();
    H2(T_{});
    H3();
    vad += SLOT;
    STAMP(49);
    barrier();
    STAMP(50);
    SBAR();
    H4(F_{});
    rescale_o();
    for_seq(std::make_integer_sequence<int, 8>{}, [&](auto IC_) {
      constexpr int I = decltype(IC_)::value;
      acc[I & 3] = mfma32(FA[I], asv(pf[I >> 2]), acc[I & 3]);
      if constexpr (I < 4) { FB[2 * I] = vread(2 * I, 1); FB[2 * I + 1] = vread(2 * I + 1, 1); }
      if constexpr (I < 5) { EXP(s1, 6 + 2 * I, ps0); EXP(s1, 7 + 2 * I, ps1); }
      if constexpr (I >= 5) {
        constexpr int c0 = (I - 5) * 3;
        CVT(s1, c0, pf[2], pf[3]); CVT(s1, c0 + 1, pf[2], pf[3]);
        if constexpr (c0 + 2 < 8) CVT(s1, c0 + 2, pf[2], pf[3]);
      }
      SBAR();
    });
#pragma unroll
    for (int I = 0; I < 8; ++I) acc[I & 3] = mfma32(FB[I], asv(pf[2 + (I >> 2)]), acc[I & 3]);
    barrier();
    {
      float l = xadd32(l_run + ps0 + ps1);
      float inv = 1.0f / l;
      char* op = ows + ((size_t)(b * 9 + 2 * wid + (r32 >> 4)) * 128 + h * 4) * 1024 + (hh * 32 + (r32 & 15)) * 16;
#pragma unroll
      for (int T = 0; T < 4; ++T)
#pragma unroll
        for (int s = 0; s < 2; ++s) {
          u32x4 w = {pk2(acc[T][8 * s + 0] * inv, acc[T][8 * s + 1] * inv), pk2(acc[T][8 * s + 2] * inv, acc[T][8 * s + 3] * inv),
                     pk2(acc[T][8 * s + 4] * inv, acc[T][8 * s + 5] * inv), pk2(acc[T][8 * s + 6] * inv, acc[T][8 * s + 7] * inv)};
          *(u32x4*)(op + T * 1024 + s * 256) = w;
        }
    }
  }
}

constexpr int K2_STAGE = 7168;
constexpr int K2_WAVE = 2 * K2_STAGE;
constexpr int K2_LDS = 8 * K2_WAVE;
__global__ __launch_bounds__(512) void k2_proj(const char* __restrict__ ows, const char* __restrict__ wo, char* __restrict__ out) {
  __shared__ __attribute__((aligned(256))) char smem[K2_LDS];
  const int lane = threadIdx.x & 63;
  const int wid = __builtin_amdgcn_readfirstlane(threadIdx.x >> 6);
  const int xcd = blockIdx.x & 7, idx = blockIdx.x >> 3;
  const int ig = 2 * (xcd & 1) + ((xcd >> 1) & 1), jg = xcd >> 2;
  const int nn = jg ? 4 : 5;
  if (idx >= 6 * nn) return;
  const int mtile = 6 * ig + idx / nn, ntile = (jg ? 5 : 0) + idx % nn;
  const unsigned lds0 = (unsigned)(size_t)(LDSP char*)smem;
  const unsigned lw = lds0 + wid * K2_WAVE;
  const char* ag = ows + ((size_t)(3 * mtile) * 128 + wid) * 1024;
  const char* bg = wo + (size_t)(32 * wid) * (DM * 2) + ntile * 128;
  const unsigned aoff = lane * 16;
  unsigned boff[4];
#pragma unroll
  for (int n = 0; n < 4; ++n) {
    const int row = 8 * n + (lane >> 3), f = ((row >> 1) & 1) | (((row >> 4) & 1) << 1);
    boff[n] = row * (DM * 2) + (((lane & 7) ^ (f << 1)) << 4);
  }
  auto issue = [&](int i, auto BC) {
    constexpr int Bf = decltype(BC)::value * K2_STAGE;
    const char* a = ag + (size_t)i * 8192;
    const char* bp = bg + (size_t)i * (256 * DM * 2);
    dma16i<false, Bf>(a, aoff, lw);
    dma16i<false, Bf + 1024>(a + 131072, aoff, lw);
    dma16i<false, Bf + 2048>(a + 262144, aoff, lw);
    dma16i<false, Bf + 3072>(bp, boff[0], lw);
    dma16i<false, Bf + 4096>(bp, boff[1], lw);
    dma16i<false, Bf + 5120>(bp, boff[2], lw);
    dma16i<false, Bf + 6144>(bp, boff[3], lw);
  };
  issue(0, IC(0));
  issue(1, IC(1));
  const int g = lane >> 4, qp = (lane >> 2) & 3, p = lane & 3;
  unsigned bad[4];
  {
    const int f = ((qp >> 1) & 1) | ((g & 1) << 1);
#pragma unroll
    for (int t = 0; t < 4; ++t) bad[t] = lw + 3072 + (16 * (g & 1) + 4 * (g >> 1) + qp) * 128 + 32 * (t ^ f) + 8 * p;
  }
  const unsigned aad = lw + lane * 16;
  f32x4 acc[3][4];
#pragma unroll
  for (int m = 0; m < 3; ++m)
#pragma unroll
    for (int t = 0; t < 4; ++t) acc[m][t] = f32x4{0.f, 0.f, 0.f, 0.f};
  auto step = [&](int i, auto BC, auto LASTC) {
    constexpr int Bf = decltype(BC)::value * K2_STAGE;
    constexpr int MODE = decltype(LASTC)::value;
    if constexpr (MODE == 2) wait_vm<0>(); else wait_vm<7>();
    s16x8 af[3], bf[4];
#pragma unroll
    for (int m = 0; m < 3; ++m) af[m] = ldsread16(aad + Bf + m * 1024);
#pragma unroll
    for (int t = 0; t < 4; ++t) bf[t] = cat(trread(bad[t] + Bf), trread(bad[t] + Bf + 1024));
    asm volatile("s_waitcnt lgkmcnt(0)" ::: "memory");
    SBAR();
    if constexpr (MODE == 0) issue(i + 2, BC);
    SBAR();
#pragma unroll
    for (int m = 0; m < 3; ++m)
#pragma unroll
      for (int t = 0; t < 4; ++t) acc[m][t] = mfma16(bf[t], af[m], acc[m][t]);
    SBAR();
  };
#pragma unroll 1
  for (int i = 0; i < 14; i += 2) {
    step(i, IC(0), IC(0));
    step(i + 1, IC(1), IC(0));
  }
  step(14, IC(0), IC(1));
  step(15, IC(1), IC(2));
  barrier();
#pragma unroll
  for (int m = 0; m < 3; ++m)
#pragma unroll
    for (int t = 0; t < 4; ++t) *(LDSP f32x4*)(size_t)(lds0 + (wid * 12 + m * 4 + t) * 1024 + lane * 16) = acc[m][t];
  barrier();
#pragma unroll
  for (int rep = 0; rep < 2; ++rep) {
    const int u = rep * 8 + wid;
    if (u < 12) {
      f32x4 sum = *(LDSP f32x4*)(size_t)(lds0 + u * 1024 + lane * 16);
#pragma unroll
      for (int w = 1; w < 8; ++w) sum += *(LDSP f32x4*)(size_t)(lds0 + (w * 12 + u) * 1024 + lane * 16);
      const int m = u >> 2, t = u & 3;
      u32x2 v = {pk2(sum[0], sum[1]), pk2(sum[2], sum[3])};
      *(u32x2*)(out + ((size_t)(mtile * 48 + m * 16 + (lane & 15)) * DM + ntile * 64 + t * 16 + 4 * g) * 2) = v;
    }
  }
}

}

extern "C" void kernel_launch(void* const* d_in, const int* in_sizes, int n_in, void* d_out, int out_size, void* d_ws, size_t ws_size,
                              hipStream_t stream) {
  const char* x = (const char*)d_in[0];
  const char* kc = (const char*)d_in[1];
  const char* vc = (const char*)d_in[2];
  const char* wq = (const char*)d_in[3];
  const char* wo = (const char*)d_in[4];
  char* ows = (char*)d_ws;
  k1_attn<<<256, 512, 0, stream>>>(x, kc, vc, wq, ows);
  k2_proj<<<256, 512, 0, stream>>>(ows, wo, (char*)d_out);
}
```

```cpp
#include <hip/hip_runtime.h>
#include <hip/hip_bf16.h>
#include <utility>

namespace {

constexpr int NB = 8, SQ = 144, DM = 576, NH = 32, SK = 512, DH = 128;

typedef __attribute__((ext_vector_type(4))) short s16x4;
typedef __attribute__((ext_vector_type(8))) short s16x8;
typedef __attribute__((ext_vector_type(4))) float f32x4;
typedef __attribute__((ext_vector_type(16))) float f32x16;
typedef __attribute__((ext_vector_type(8))) __bf16 bf16x8;
typedef __attribute__((ext_vector_type(2))) __bf16 bf16x2;
typedef __attribute__((ext_vector_type(2))) float f32x2;
typedef __attribute__((ext_vector_type(4))) unsigned u32x4;
typedef __attribute__((ext_vector_type(2))) unsigned u32x2;
#define LDSP __attribute__((address_space(3)))
#define DI __device__ __forceinline__

DI f32x4 mfma16(s16x8 a, s16x8 b, f32x4 c) {
  return __builtin_amdgcn_mfma_f32_16x16x32_bf16(__builtin_bit_cast(bf16x8, a), __builtin_bit_cast(bf16x8, b), c, 0, 0, 0);
}
DI f32x4 mfma16k16(s16x4 a, s16x4 b, f32x4 c) { return __builtin_amdgcn_mfma_f32_16x16x16bf16_1k(a, b, c, 0, 0, 0); }
DI f32x16 mfma32(s16x8 a, s16x8 b, f32x16 c) {
  return __builtin_amdgcn_mfma_f32_32x32x16_bf16(__builtin_bit_cast(bf16x8, a), __builtin_bit_cast(bf16x8, b), c, 0, 0, 0);
}
DI unsigned pk2(float lo, float hi) {
  f32x2 v = {lo, hi};
  bf16x2 r = __builtin_convertvector(v, bf16x2);
  return __builtin_bit_cast(unsigned, r);
}
DI s16x4 trread(unsigned addr) { return __builtin_amdgcn_ds_read_tr16_b64_v4i16((LDSP s16x4*)(size_t)addr); }
DI s16x8 ldsread16(unsigned addr) { return *(LDSP s16x8*)(size_t)addr; }
DI s16x8 cat(s16x4 a, s16x4 b) { return __builtin_shufflevector(a, b, 0, 1, 2, 3, 4, 5, 6, 7); }
DI float max3(float a, float b, float c) { return __builtin_fmaxf(__builtin_fmaxf(a, b), c); }
DI float xmax32(float v) {
  auto r = __builtin_amdgcn_permlane32_swap(__float_as_uint(v), __float_as_uint(v), false, false);
  return fmaxf(__uint_as_float(r[0]), __uint_as_float(r[1]));
}
DI float xmax16(float v) {
  auto r = __builtin_amdgcn_permlane16_swap(__float_as_uint(v), __float_as_uint(v), false, false);
  return fmaxf(__uint_as_float(r[0]), __uint_as_float(r[1]));
}
DI float xadd32(float v) {
  auto r = __builtin_amdgcn_permlane32_swap(__float_as_uint(v), __float_as_uint(v), false, false);
  return __uint_as_float(r[0]) + __uint_as_float(r[1]);
}
DI float xadd16(float v) {
  auto r = __builtin_amdgcn_permlane16_swap(__float_as_uint(v), __float_as_uint(v), false, false);
  return __uint_as_float(r[0]) + __uint_as_float(r[1]);
}

template <bool NT>
DI void dma16(const char* sbase, unsigned voff, unsigned lds) {
  if constexpr (NT)
    asm volatile("s_mov_b32 m0, %1\n\ts_nop 0\n\tglobal_load_lds_dwordx4 %0, %2 nt" ::"v"(voff), "s"(lds), "s"(sbase) : "memory");
  else
    asm volatile("s_mov_b32 m0, %1\n\ts_nop 0\n\tglobal_load_lds_dwordx4 %0, %2" ::"v"(voff), "s"(lds), "s"(sbase) : "memory");
}
DI void dma16_lo(const char* sbase, unsigned voff, unsigned lds) {
  asm volatile("s_mov_b32 m0, %1\n\ts_mov_b32 exec_hi, 0\n\tglobal_load_lds_dwordx4 %0, %2\n\ts_mov_b32 exec_hi, -1" ::"v"(voff), "s"(lds), "s"(sbase) : "memory");
}
template <bool NT, int IMM>
DI void dma16i(const char* sbase, unsigned voff, unsigned lds) {
  if constexpr (NT)
    asm volatile("s_add_u32 m0, %1, %3\n\ts_nop 0\n\tglobal_load_lds_dwordx4 %0, %2 nt" ::"v"(voff), "s"(lds), "s"(sbase), "i"(IMM) : "memory", "scc");
  else
    asm volatile("s_add_u32 m0, %1, %3\n\ts_nop 0\n\tglobal_load_lds_dwordx4 %0, %2" ::"v"(voff), "s"(lds), "s"(sbase), "i"(IMM) : "memory", "scc");
}
template <int IMM>
DI void dma16i_lo(const char* sbase, unsigned voff, unsigned lds) {
  asm volatile("s_add_u32 m0, %1, %3\n\ts_mov_b32 exec_hi, 0\n\tglobal_load_lds_dwordx4 %0, %2\n\ts_mov_b32 exec_hi, -1" ::"v"(voff), "s"(lds), "s"(sbase), "i"(IMM) : "memory", "scc");
}
template <int N> DI void wait_vm() { asm volatile("s_waitcnt vmcnt(%0)" ::"i"(N) : "memory"); }
#define STAMP(i)
DI void barrier() { asm volatile("s_waitcnt lgkmcnt(0)\n\ts_barrier" ::: "memory"); }
DI void barrier_nw() { asm volatile("s_barrier" ::: "memory"); }
template <int... I, class F> DI void for_seq(std::integer_sequence<int, I...>, F&& f) { (f(std::integral_constant<int, I>{}), ...); }
#define IC(n) std::integral_constant<int, n>{}
#define SGB(mask, n) __builtin_amdgcn_sched_group_barrier(mask, n, 0)
#define SBAR() __builtin_amdgcn_sched_barrier(0)

#define KV_NT true
#define LOOKAHEAD 4
#define OSTORE(p, v) (*(p) = (v))
#define K2A_NT false
#define K2B_NT false
#define Q_NT false
#define HPRIO 2
#define EARLYW -1

constexpr int SLOT = 17408;
constexpr int GRP = 1088;
constexpr int QRING = 3;
constexpr int QPOS0 = 3 * SLOT;
constexpr int QX_OFF = 9 * SLOT;
constexpr int ST_OFF = QX_OFF + 4096;
constexpr int SY_OFF = ST_OFF + 512;
constexpr int RDYQ = SY_OFF, RDYT = SY_OFF + 16, DONEC = SY_OFF + 32, DONEH = SY_OFF + 48, AUXF = SY_OFF + 64;
constexpr int DUMMY_OFF = SY_OFF + 128;
constexpr int STAMP_OFF = DUMMY_OFF + 1024;
constexpr int K1_LDS = STAMP_OFF + 512;
constexpr int CMB_OFF = 7 * SLOT;
constexpr float CSC = 0.08838834764831845f * 1.4426950408889634f;
constexpr float THR = 8.0f;

constexpr int NEV = 19;
constexpr int need_ev(int it) {
  if (it < 9) return it;
  int a = it - 9;
  return a < 2 ? 9 + a : (a == 15 ? 18 : 11 + (a - 2) / 2);
}
constexpr int free_ev(int it) { return it < 9 ? it + 1 : need_ev(it) + 2; }
struct Sched { int n; int item[48]; int when[48]; };
#define WOPF_EV 16
#define LA2 4
#define EARLYT 0
constexpr Sched make_sched() {
  Sched S{};
  int n = 0;
  auto add = [&](int it, int w) { S.item[n] = it; S.when[n] = w; ++n; };
  add(0, -1); add(1, -1);
  for (int a = 0; a < EARLYT; ++a) add(9 + a, EARLYW);
  int nt = EARLYT;
  for (int e = 0; e < NEV; ++e) {
    if (e <= 6) add(e + 2, e);
    int lim = (e == 7) ? 2 : (e == 8 ? 4 : (e == 9 ? LA2 + 1 : (e == 10 ? LA2 + 2 : (e >= 11 ? 2 * (e - 11) + 4 + LA2 : 0))));
    if (lim > 16) lim = 16;
    for (; nt < lim; ++nt) add(9 + nt, e);
    if (e == WOPF_EV) for (int k = 0; k < 5; ++k) add(100 + k, e);
  }
  for (int i = 1; i < n; ++i)
    for (int j = i; j > 0 && S.when[j - 1] > S.when[j]; --j) {
      int t = S.when[j]; S.when[j] = S.when[j - 1]; S.when[j - 1] = t;
      t = S.item[j]; S.item[j] = S.item[j - 1]; S.item[j - 1] = t;
    }
  S.n = n;
  return S;
}
constexpr Sched SCH = make_sched();
constexpr int item_ops(int it) { return it < 9 ? 9 : (it < 100 ? 4 : 1); }
constexpr int sched_wait(int ev) {
  int pos = -1, cnt = 0;
  for (int e = 0; e < SCH.n; ++e) if (SCH.item[e] < 100 && need_ev(SCH.item[e]) == ev) pos = e;
  for (int e = pos + 1; e < SCH.n; ++e) if (SCH.when[e] < ev) cnt += item_ops(SCH.item[e]);
  return cnt;
}
constexpr bool sched_ok() {
  for (int e = 0; e < SCH.n; ++e) {
    int it = SCH.item[e], w = SCH.when[e];
    if (it >= 100) continue;
    if (w >= need_ev(it)) return false;
    int prev = -1;
    if (it < 9) { if (it >= QRING) prev = it - QRING; }
    else { int a = it - 9; if (a >= 9) prev = it - 9; else if (a >= 3) prev = 6 + (a - 3) / 2; }
    if (prev >= 0 && w < free_ev(prev)) return false;
  }
  for (int ev = 0; ev < NEV; ++ev) if (sched_wait(ev) > 60) return false;
  return true;
}
static_assert(sched_ok(), "loader schedule violates a slot or ordering constraint");

__global__ __launch_bounds__(512) void k1_attn(const char* __restrict__ x, const char* __restrict__ kc, const char* __restrict__ vc,
                                               const char* __restrict__ wq, const char* __restrict__ wo, char* __restrict__ ows) {
  __shared__ __attribute__((aligned(256))) char smem[K1_LDS];
  const int tid = threadIdx.x;
  const int lane = tid & 63;
  const int wid = __builtin_amdgcn_readfirstlane(tid >> 6);
  const int xcd = blockIdx.x & 7, idx = blockIdx.x >> 3;
  const int b = 4 * (xcd & 1) + (idx & 3);
  const int h = 8 * (xcd >> 1) + (idx >> 2);
  const unsigned lds0 = (unsigned)(size_t)(LDSP char*)smem;
  STAMP(0);

  if (wid >= 4) {
    const int hi = wid - 4;
    const char* xg = x + (size_t)(b * SQ) * (DM * 2);
    const char* wg = wq + h * 256;
    const char* kg = kc + (size_t)(b * NH + h) * (SK * DH * 2);
    const char* vg = vc + (size_t)(b * NH + h) * (SK * DH * 2);
    unsigned xoff[4], xoffe, woff[4], koff[4], voff[4];
    {
      const int cc = lane & 7, c16 = lane & 15;
#pragma unroll
      for (int n = 0; n < 4; ++n) {
        int rx = hi * 32 + n * 8 + (lane >> 3);
        xoff[n] = rx * (DM * 2) + ((cc ^ ((rx >> 1) & 7)) << 4);
        int r = hi * 16 + n * 4 + (lane >> 4);
        woff[n] = r * 8192 + ((c16 ^ (((r & 3) << 2) | (((r >> 3) & 1) << 1))) << 4);
        koff[n] = r * 256 + ((c16 ^ (r & 3)) << 4);
        voff[n] = r * 256 + (c16 << 4);
      }
      int rx = 128 + hi * 4 + ((lane >> 3) & 3);
      xoffe = rx * (DM * 2) + ((cc ^ ((rx >> 1) & 7)) << 4);
    }
    const unsigned lq = lds0 + QPOS0 + hi * 4096;
    const unsigned lqe = lds0 + QPOS0 + 32768 + hi * 512;
    const unsigned lt = lds0 + hi * (4 * GRP);
    auto issue_q = [&](auto SC) {
      constexpr int S = decltype(SC)::value;
      constexpr int P = (S % 3) * (2 * SLOT);
      const char* ws = wg + (size_t)S * (64 * 8192);
      const char* xs = xg + S * 128;
      dma16i<Q_NT, P>(ws, woff[0], lq); dma16i<Q_NT, P + 1024>(ws, woff[1], lq);
      dma16i<Q_NT, P + 2048>(ws, woff[2], lq); dma16i<Q_NT, P + 3072>(ws, woff[3], lq);
      dma16i<Q_NT, P + 16384>(xs, xoff[0], lq); dma16i<Q_NT, P + 16384 + 1024>(xs, xoff[1], lq);
      dma16i<Q_NT, P + 16384 + 2048>(xs, xoff[2], lq); dma16i<Q_NT, P + 16384 + 3072>(xs, xoff[3], lq);
      dma16i_lo<P>(xs, xoffe, lqe);
    };
    auto issue_t = [&](auto AC) {
      constexpr int A = decltype(AC)::value;
      constexpr int L = (A % 9) * SLOT;
      constexpr bool isK = (A == 0) || ((A & 1) && A != 15);
      if constexpr (isK) {
        const char* sp = kg + ((A + 1) >> 1) * 16384;
        dma16i<KV_NT, L>(sp, koff[0], lt); dma16i<KV_NT, L + GRP>(sp, koff[1], lt);
        dma16i<KV_NT, L + 2 * GRP>(sp, koff[2], lt); dma16i<KV_NT, L + 3 * GRP>(sp, koff[3], lt);
      } else {
        const char* sp = vg + ((A == 15) ? 7 : ((A - 2) >> 1)) * 16384;
        dma16i<KV_NT, L>(sp, voff[0], lt); dma16i<KV_NT, L + GRP>(sp, voff[1], lt);
        dma16i<KV_NT, L + 2 * GRP>(sp, voff[2], lt); dma16i<KV_NT, L + 3 * GRP>(sp, voff[3], lt);
      }
    };
    const char* wop = wo + (size_t)blockIdx.x * 18432 + hi * 4608;
    auto issue_at = [&](auto CC) {
      constexpr int C = decltype(CC)::value;
      for_seq(std::make_integer_sequence<int, SCH.n>{}, [&](auto EC) {
        constexpr int E = decltype(EC)::value;
        if constexpr (SCH.when[E] == C) {
          constexpr int IT = SCH.item[E];
          if constexpr (IT < 9) issue_q(IC(IT));
          else if constexpr (IT < 100) issue_t(IC(IT - 9));
          else if constexpr (IT < 104) dma16i<false, DUMMY_OFF>(wop + (IT - 100) * 1024, lane * 16, lds0);
          else dma16i_lo<DUMMY_OFF>(wop + 4096, lane * 16, lds0);
        }
      });
    };
    issue_at(IC(-1));

    const int r16 = lane & 15, g = lane >> 4, qp = (lane >> 2) & 3, p = lane & 3;
    unsigned wbase[2], xbase[2], kb, vb;
    {
      const int ew = (qp << 1) | (g & 1);
#pragma unroll
      for (int t = 0; t < 2; ++t) wbase[t] = lds0 + QPOS0 + (8 * g + qp) * 256 + 32 * ((2 * hi + t) ^ ew) + 8 * p;
      const int rx = 128 + r16;
#pragma unroll
      for (int s = 0; s < 2; ++s) xbase[s] = lds0 + QPOS0 + 16384 + rx * 128 + (((4 * s + g) ^ ((rx >> 1) & 7)) << 4);
      kb = lds0 + (4 * hi + (r16 & 3)) * GRP + (r16 >> 2) * 256 + ((g ^ (r16 >> 2)) << 4);
      vb = lds0 + (4 * hi + qp) * GRP + g * 256 + 8 * p;
    }
    __builtin_amdgcn_s_setprio(HPRIO);
    f32x4 qa[2] = {f32x4{0.f, 0.f, 0.f, 0.f}, f32x4{0.f, 0.f, 0.f, 0.f}};
    for_seq(std::make_integer_sequence<int, 9>{}, [&](auto SC) {
      constexpr int S = decltype(SC)::value;
      wait_vm<sched_wait(S)>();
      if constexpr (S == 0) STAMP(1);
      barrier();
      constexpr unsigned P = (S % 3) * (2 * SLOT);
      s16x8 xb[2], wf[2][2];
#pragma unroll
      for (int ks = 0; ks < 2; ++ks) {
        xb[ks] = ldsread16(xbase[ks] + P);
#pragma unroll
        for (int t = 0; t < 2; ++t) wf[ks][t] = cat(trread(wbase[t] + P + ks * 8192), trread(wbase[t] + P + ks * 8192 + 1024));
      }
      SBAR();
      issue_at(SC);
      SBAR();
#pragma unroll
      for (int ks = 0; ks < 2; ++ks)
#pragma unroll
        for (int t = 0; t < 2; ++t) {
          qa[t] = mfma16(wf[ks][t], xb[ks], qa[t]);
        }
    });
    {
      qa[0] *= CSC; qa[1] *= CSC;
      unsigned a0 = pk2(qa[0][0], qa[0][1]), a1 = pk2(qa[0][2], qa[0][3]);
      unsigned b0 = pk2(qa[1][0], qa[1][1]), b1 = pk2(qa[1][2], qa[1][3]);
      auto r0 = __builtin_amdgcn_permlane32_swap(a0, b0, false, false);
      auto r1 = __builtin_amdgcn_permlane32_swap(a1, b1, false, false);
      auto t0 = __builtin_amdgcn_permlane16_swap(r0[0], r0[1], false, false);
      auto t1 = __builtin_amdgcn_permlane16_swap(r1[0], r1[1], false, false);
      u32x4 w = {t0[0], t1[0], t0[1], t1[1]};
      *(LDSP u32x4*)(size_t)(lds0 + QX_OFF + hi * 1024 + lane * 16) = w;
    }
    STAMP(2);
    s16x8 qf[4];
    float m_run = -__builtin_inff(), l_run = 0.f, al_pend = 1.f;
    f32x4 oa[8];
#pragma unroll
    for (int T = 0; T < 8; ++T) oa[T] = f32x4{0.f, 0.f, 0.f, 0.f};
    s16x4 p_cur = {0, 0, 0, 0}, p_nxt = {0, 0, 0, 0};
    bool hpend = false;
    auto kstep = [&](const s16x8 (&kf)[4], bool first) {
      f32x4 s = {0.f, 0.f, 0.f, 0.f};
#pragma unroll
      for (int ks = 0; ks < 4; ++ks) s = mfma16(kf[ks], qf[ks], s);
      float mx = max3(fmaxf(s[0], s[1]), s[2], s[3]);
      mx = xmax32(xmax16(mx));
      if (first) {
        m_run = mx;
      } else if (__builtin_expect(__any(mx > m_run + THR), 0)) {
        float m_new = fmaxf(m_run, mx);
        al_pend = __builtin_amdgcn_exp2f(m_run - m_new);
        m_run = m_new;
        l_run *= al_pend;
        hpend = true;
      }
#pragma unroll
      for (int r = 0; r < 4; ++r) { s[r] = __builtin_amdgcn_exp2f(s[r] - m_run); l_run += s[r]; }
      u32x2 w = {pk2(s[0], s[1]), pk2(s[2], s[3])};
      p_nxt = __builtin_bit_cast(s16x4, w);
      if (first) p_cur = p_nxt;
    };
    auto vstep = [&](const s16x4 (&vf)[8], bool last) {
#pragma unroll
      for (int T = 0; T < 8; ++T) oa[T] = mfma16k16(vf[T], p_cur, oa[T]);
      if (!last && __builtin_expect(hpend, 0)) {
#pragma unroll
        for (int T = 0; T < 8; ++T) oa[T] *= al_pend;
        hpend = false;
      }
      p_cur = p_nxt;
    };
    for_seq(std::make_integer_sequence<int, 10>{}, [&](auto EC) {
      constexpr int EV = 9 + decltype(EC)::value;
      wait_vm<sched_wait(EV)>();
      if constexpr (EV == 9) STAMP(3);
      if constexpr (EV == 13) STAMP(4);
      if constexpr (EV == 18) STAMP(5);
      barrier();
      constexpr int AV = (EV >= 11) ? (EV == 18 ? 15 : 2 * (EV - 11) + 2) : -1;
      constexpr int AK = (EV == 9) ? 0 : (EV == 10 ? 1 : (EV <= 16 ? 2 * (EV - 11) + 3 : -1));
      if constexpr (EV == 9) {
#pragma unroll
        for (int s = 0; s < 4; ++s) qf[s] = ldsread16(lds0 + QX_OFF + s * 1024 + lane * 16);
      }
      s16x8 kf[4]; s16x4 vf[8];
      if constexpr (AV >= 0) {
#pragma unroll
        for (int T = 0; T < 8; ++T) vf[T] = trread(vb + (AV % 9) * SLOT + T * 32);
      }
      if constexpr (AK >= 0) {
#pragma unroll
        for (int ks = 0; ks < 4; ++ks) kf[ks] = ldsread16(kb + (AK % 9) * SLOT + ks * 64);
      }
      SBAR();
      issue_at(IC(EV));
      SBAR();
      if constexpr (AV >= 0) vstep(vf, AV == 15);
      if constexpr (AK >= 0) kstep(kf, AK == 0);
    });
    {
      float l = xadd32(xadd16(l_run));
      if (g == 0) {
        f32x2 st = {m_run, l};
        *(LDSP f32x2*)(size_t)(lds0 + ST_OFF + (hi * 16 + r16) * 8) = st;
      }
#pragma unroll
      for (int T = 0; T < 8; ++T) *(LDSP f32x4*)(size_t)(lds0 + CMB_OFF + (hi * 8 + T) * 1024 + lane * 16) = oa[T];
    }
    barrier();
    {
      f32x2 st[4];
#pragma unroll
      for (int k = 0; k < 4; ++k) st[k] = *(LDSP f32x2*)(size_t)(lds0 + ST_OFF + (k * 16 + r16) * 8);
      float m = fmaxf(fmaxf(st[0][0], st[1][0]), fmaxf(st[2][0], st[3][0]));
      float sc[4], l = 0.f;
#pragma unroll
      for (int k = 0; k < 4; ++k) { sc[k] = __builtin_amdgcn_exp2f(st[k][0] - m); l += sc[k] * st[k][1]; }
      float inv = 1.0f / l;
      const int T = 2 * hi + (g & 1);
      f32x4 lo = {0.f, 0.f, 0.f, 0.f}, hi4 = {0.f, 0.f, 0.f, 0.f};
#pragma unroll
      for (int k = 0; k < 4; ++k) {
        unsigned base = lds0 + CMB_OFF + (k * 8 + T) * 1024 + r16 * 16;
        f32x4 a = *(LDSP f32x4*)(size_t)(base + (g >> 1) * 256);
        f32x4 c2 = *(LDSP f32x4*)(size_t)(base + (2 + (g >> 1)) * 256);
        lo += a * sc[k];
        hi4 += c2 * sc[k];
      }
      u32x4 w = {pk2(lo[0] * inv, lo[1] * inv), pk2(lo[2] * inv, lo[3] * inv), pk2(hi4[0] * inv, hi4[1] * inv), pk2(hi4[2] * inv, hi4[3] * inv)};
      OSTORE((u32x4*)(ows + ((size_t)(b * 9 + 8) * 128 + h * 4 + hi) * 1024 + lane * 16), w);
    }
  } else {
    const int r32 = lane & 31, hh = lane >> 5, cb = (lane >> 4) & 1, qp = (lane >> 2) & 3, p = lane & 3;
    f32x16 acc[4];
#pragma unroll
    for (int T = 0; T < 4; ++T)
#pragma unroll
      for (int r = 0; r < 16; ++r) acc[T][r] = 0.f;
    s16x8 FA[8], FB[8];
    using T_ = std::true_type; using F_ = std::false_type;
    {
      const char* wp = wq + (size_t)blockIdx.x * 18432 + wid * 4608 + lane * 16;
      u32x4 t0 = *(const u32x4*)wp, t1 = *(const u32x4*)(wp + 1024), t2 = *(const u32x4*)(wp + 2048), t3 = *(const u32x4*)(wp + 3072);
      u32x4 t4 = *(const u32x4*)(wp + 4096 - (lane >> 5) * 512);
      const char* xp = x + (size_t)blockIdx.x * 5184 + wid * 1296 + lane * 16;
      u32x4 t5 = *(const u32x4*)xp, t6 = *(const u32x4*)(xp + 272);
      asm volatile("" ::"v"(t0), "v"(t1), "v"(t2), "v"(t3), "v"(t4), "v"(t5), "v"(t6));
    }

    {
      unsigned wad[4], xad[4];
#pragma unroll
      for (int T = 0; T < 4; ++T) {
        unsigned ch = ((4 * T + 2 * cb + (p >> 1)) ^ ((qp << 2) | (hh << 1)));
        wad[T] = lds0 + QPOS0 + (8 * hh + qp) * 256 + ch * 16 + 8 * (p & 1);
      }
      const int rx = wid * 32 + r32;
#pragma unroll
      for (int ks = 0; ks < 4; ++ks) xad[ks] = lds0 + QPOS0 + 16384 + rx * 128 + (((2 * ks + hh) ^ ((rx >> 1) & 7)) << 4);
      s16x8 xa[2], xb[2];
      auto qhalf = [&](s16x8 (&CUR)[8], s16x8 (&xc)[2], s16x8 (&NXT)[8], s16x8 (&xn)[2], auto HC, auto DOMM, auto DOLD) {
        constexpr int HALF = decltype(HC)::value;
        auto rdw = [&](int F) {
          const int k = F >> 2, T = F & 3;
          NXT[F] = cat(trread(wad[T] + (2 * HALF + k) * 4096), trread(wad[T] + (2 * HALF + k) * 4096 + 1024));
        };
        for_seq(std::make_integer_sequence<int, 8>{}, [&](auto IC_) {
          constexpr int I = decltype(IC_)::value;
          if constexpr (decltype(DOMM)::value) { acc[I & 3] = mfma32(CUR[I], xc[I >> 2], acc[I & 3]); SBAR(); }
          if constexpr (decltype(DOLD)::value) {
            if constexpr (I == 0) { xn[0] = ldsread16(xad[2 * HALF]); rdw(0); }
            if constexpr (I == 1) { rdw(1); rdw(2); }
            if constexpr (I == 2) { rdw(3); xn[1] = ldsread16(xad[2 * HALF + 1]); }
            if constexpr (I == 3) { rdw(4); rdw(5); }
            if constexpr (I == 4) { rdw(6); }
            if constexpr (I == 5) { rdw(7); }
          }
          SBAR();
        });
      };
      barrier();
      STAMP(1);
      qhalf(FB, xb, FA, xa, IC(0), F_{}, T_{});
      qhalf(FA, xa, FB, xb, IC(1), T_{}, T_{});
      int pos = 0;
#pragma unroll 1
      for (int S = 0; S < 8; ++S) {
        const unsigned d = (pos == QRING - 1) ? (unsigned)(-2 * (QRING - 1) * SLOT) : (unsigned)(2 * SLOT);
        pos = (pos == QRING - 1) ? 0 : pos + 1;
#pragma unroll
        for (int T = 0; T < 4; ++T) { wad[T] += d; xad[T] += d; }
        asm volatile("" : "+v"(wad[0]), "+v"(wad[1]), "+v"(wad[2]), "+v"(wad[3]), "+v"(xad[0]), "+v"(xad[1]), "+v"(xad[2]), "+v"(xad[3]));
        barrier();
        SBAR();
        qhalf(FB, xb, FA, xa, IC(0), T_{}, T_{});
        qhalf(FA, xa, FB, xb, IC(1), T_{}, T_{});
      }
      qhalf(FB, xb, FA, xa, IC(0), T_{}, F_{});
    }
    STAMP(2);

    s16x8 qf[8];
#pragma unroll
    for (int T = 0; T < 4; ++T) {
      acc[T] *= CSC;
#pragma unroll
      for (int s = 0; s < 2; ++s) {
        unsigned a0 = pk2(acc[T][8 * s + 0], acc[T][8 * s + 1]), a1 = pk2(acc[T][8 * s + 2], acc[T][8 * s + 3]);
        unsigned b0 = pk2(acc[T][8 * s + 4], acc[T][8 * s + 5]), b1 = pk2(acc[T][8 * s + 6], acc[T][8 * s + 7]);
        auto r0 = __builtin_amdgcn_permlane32_swap(a0, b0, false, false);
        auto r1 = __builtin_amdgcn_permlane32_swap(a1, b1, false, false);
        u32x4 w = {r0[0], r1[0], r0[1], r1[1]};
        qf[2 * T + s] = __builtin_bit_cast(s16x8, w);
      }
    }
#pragma unroll
    for (int T = 0; T < 4; ++T)
#pragma unroll
      for (int r = 0; r < 16; ++r) acc[T][r] = 0.f;

    unsigned kad[2], vad;
    {
      const int a_ = r32 >> 3, h_ = (r32 >> 2) & 1, j_ = r32 & 3;
      const int grp = 4 * (a_ >> 1) + j_, rin = 2 * (a_ & 1) + h_;
#pragma unroll
      for (int e = 0; e < 2; ++e) kad[e] = lds0 + grp * GRP + rin * 256 + (((2 * e + hh) ^ rin) << 4);
      vad = lds0 + qp * GRP + hh * 256 + 32 * cb + 8 * p;
    }
    f32x16 s0, s1, negm;
    u32x4 pf[4];
    float l_run = 0.f, al_pend = 1.f, mx = 0.f, ps0 = 0.f, ps1 = 0.f;
    bool pend = false;
#define PIN1(a) asm volatile("" : "+v"(a))
#define PIN2(a, b) asm volatile("" : "+v"(a), "+v"(b))
#define PIN4(a, b, c, d) asm volatile("" : "+v"(a), "+v"(b), "+v"(c), "+v"(d))
    auto kread = [&](int F, int t) { return ldsread16(kad[F & 1] + t * (8 * GRP) + (F >> 1) * 64); };
    auto vread = [&](int F, int hf) {
      const unsigned o = (8 * hf + 4 * (F >> 2)) * GRP + 64 * (F & 3);
      return cat(trread(vad + o), trread(vad + o + 512));
    };
    auto EXP2 = [&](f32x16& s, int r) {
      float e0 = __builtin_amdgcn_exp2f(s[r]), e1 = __builtin_amdgcn_exp2f(s[r + 1]);
      PIN2(e0, e1);
      s[r] = e0; s[r + 1] = e1;
    };
    auto ADD2 = [&](const f32x16& s, int q) {
      ps0 += s[q]; ps1 += s[q + 1]; PIN2(ps0, ps1); };
    auto CVT2 = [&](const f32x16& s, int i, u32x4& lo, u32x4& hi2) {
      unsigned w0 = pk2(s[2 * i], s[2 * i + 1]), w1 = pk2(s[2 * i + 2], s[2 * i + 3]);
      PIN2(w0, w1);
      if (i < 4) { lo[i] = w0; lo[i + 1] = w1; } else { hi2[i - 4] = w0; hi2[i - 3] = w1; }
    };
    auto asv = [](const u32x4& v) { return __builtin_bit_cast(s16x8, v); };
    auto fin1 = [&](auto IC_) {
      constexpr int I = decltype(IC_)::value;
      if constexpr (I < 7) ADD2(s1, 2 + 2 * I);
      if constexpr (I < 6) EXP2(s1, 4 + 2 * I);
      if constexpr (I == 6) { CVT2(s1, 0, pf[2], pf[3]); CVT2(s1, 2, pf[2], pf[3]); }
      if constexpr (I == 7) { CVT2(s1, 4, pf[2], pf[3]); CVT2(s1, 6, pf[2], pf[3]); }
    };

    auto H1 = [&](auto FIN) {
      for_seq(std::make_integer_sequence<int, 8>{}, [&](auto IC_) {
        constexpr int I = decltype(IC_)::value;
        if constexpr (I == 0) s0 = mfma32(FA[0], qf[0], negm); else s0 = mfma32(FA[I], qf[I], s0);
        SBAR();
        FB[I] = kread(I, 1);
        if constexpr (decltype(FIN)::value) fin1(IC_);
        SBAR();
      });
    };
    auto H2 = [&](auto RDV) {
      for_seq(std::make_integer_sequence<int, 8>{}, [&](auto IC_) {
        constexpr int I = decltype(IC_)::value;
        if constexpr (I == 0) s1 = mfma32(FB[0], qf[0], negm); else s1 = mfma32(FB[I], qf[I], s1);
        SBAR();
        if constexpr (decltype(RDV)::value) FA[I] = vread(I, 0);
        if constexpr (I == 4) { mx = max3(s0[0], s0[1], s0[2]); mx = max3(mx, s0[3], s0[4]); PIN1(mx); }
        if constexpr (I == 5) { mx = max3(mx, s0[5], s0[6]); mx = max3(mx, s0[7], s0[8]); PIN1(mx); }
        if constexpr (I == 6) { mx = max3(mx, s0[9], s0[10]); mx = max3(mx, s0[11], s0[12]); PIN1(mx); }
        if constexpr (I == 7) { mx = max3(mx, s0[13], s0[14]); mx = max3(mx, s0[15], s0[15]); PIN1(mx); }
        SBAR();
      });
    };
    auto rescale_o = [&]() {
      if (__builtin_expect(pend, 0)) {
#pragma unroll
        for (int T = 0; T < 4; ++T) acc[T] *= al_pend;
        pend = false;
      }
    };
    auto H3 = [&]() {
      for_seq(std::make_integer_sequence<int, 4>{}, [&](auto IC_) {
        constexpr int I = decltype(IC_)::value;
        acc[I & 3] = mfma32(FA[I], asv(pf[I >> 2]), acc[I & 3]);
        SBAR();
        FB[I] = vread(I, 1);
        if constexpr (I == 2) {
          mx = max3(mx, s1[0], s1[1]); mx = max3(mx, s1[2], s1[3]); mx = max3(mx, s1[4], s1[5]); mx = max3(mx, s1[6], s1[7]); PIN1(mx);
        }
        if constexpr (I == 3) {
          mx = max3(mx, s1[8], s1[9]); mx = max3(mx, s1[10], s1[11]); mx = max3(mx, s1[12], s1[13]); mx = max3(mx, s1[14], s1[15]); PIN1(mx);
        }
        SBAR();
      });
      mx = xmax32(mx);
      if (__builtin_expect(__any(mx > THR), 0)) {
        float d = fmaxf(mx, 0.f);
        al_pend = __builtin_amdgcn_exp2f(-d);
        pend = true;
        l_run = (l_run + ps0 + ps1) * al_pend;
        ps0 = 0.f; ps1 = 0.f;
#pragma unroll
        for (int r = 0; r < 16; ++r) { negm[r] -= d; s0[r] -= d; s1[r] -= d; }
      }
      SBAR();
      for_seq(std::make_integer_sequence<int, 4>{}, [&](auto IC_) {
        constexpr int I = 4 + decltype(IC_)::value;
        acc[I & 3] = mfma32(FA[I], asv(pf[I >> 2]), acc[I & 3]);
        SBAR();
        FB[I] = vread(I, 1);
        if constexpr (I > 4) ADD2(s0, 2 * (I - 5));
        EXP2(s0, 2 * (I - 4));
        SBAR();
      });
    };
    auto H4 = [&](auto NEXTK) {
      for_seq(std::make_integer_sequence<int, 8>{}, [&](auto IC_) {
        constexpr int I = decltype(IC_)::value;
        acc[I & 3] = mfma32(FB[I], asv(pf[2 + (I >> 2)]), acc[I & 3]);
        SBAR();
        if constexpr (decltype(NEXTK)::value) FA[I] = kread(I, 0);
        if constexpr (I < 5) ADD2(s0, 6 + 2 * I);
        if constexpr (I < 4) EXP2(s0, 8 + 2 * I);
        if constexpr (I >= 4) CVT2(s0, 2 * (I - 4), pf[0], pf[1]);
        if constexpr (I == 5) EXP2(s1, 0);
        if constexpr (I == 6) EXP2(s1, 2);
        if constexpr (I == 7) ADD2(s1, 0);
        SBAR();
      });
    };
    auto adv = [&](unsigned& a, int& slot) {
      slot += 2;
      unsigned d = 2 * SLOT;
      if (slot >= 9) { slot -= 9; d = (unsigned)(-7 * SLOT); }
      a += d;
    };

#pragma unroll
    for (int r = 0; r < 16; ++r) negm[r] = 0.f;
    barrier();
    STAMP(3);
#pragma unroll
    for (int F = 0; F < 8; ++F) FA[F] = kread(F, 0);
    SBAR();
    H1(F_{});
    H2(F_{});
#pragma unroll
    for (int i = 0; i < 8; ++i) mx = max3(mx, s1[2 * i], s1[2 * i + 1]);
    mx = xmax32(mx);
#pragma unroll
    for (int r = 0; r < 16; ++r) { negm[r] = -mx; s0[r] -= mx; s1[r] -= mx; }
    int kslot = 1, vslot = 2, kslot2 = 1;
    kad[0] += SLOT; kad[1] += SLOT; vad += 2 * SLOT;
    barrier_nw();
#pragma unroll
    for (int F = 0; F < 8; ++F) FA[F] = kread(F, 0);
#pragma unroll
    for (int r = 0; r < 16; r += 2) EXP2(s0, r);
#pragma unroll
    for (int r = 0; r < 16; r += 2) ADD2(s0, r);
#pragma unroll
    for (int i = 0; i < 8; i += 2) CVT2(s0, i, pf[0], pf[1]);
    EXP2(s1, 0); EXP2(s1, 2); ADD2(s1, 0);
    SBAR();
    H1(T_{});
#pragma unroll 1
    for (int j = 0; j < 6; ++j) {
      barrier_nw();
      SBAR();
      H2(T_{});
      H3();
      adv(kad[0], kslot); adv(kad[1], kslot2); adv(vad, vslot);
      SBAR();
      H4(T_{});
      H1(T_{});
      rescale_o();
    }
    barrier_nw();
    SBAR();
    H2(T_{});
    H3();
    H4(F_{});
    for_seq(std::make_integer_sequence<int, 8>{}, fin1);
    rescale_o();
    const float inv = 1.0f / xadd32(l_run + ps0 + ps1);
    vad += SLOT;
    char* op = ows + ((size_t)(b * 9 + 2 * wid + (r32 >> 4)) * 128 + h * 4) * 1024 + (hh * 32 + (r32 & 15)) * 16;
    STAMP(5);
    barrier_nw();
    STAMP(6);
    SBAR();
    {
      auto vr = [&](int T, int u) {
        const unsigned o = (4 * u) * GRP + 64 * T;
        return cat(trread(vad + o), trread(vad + o + 512));
      };
#pragma unroll
      for (int F = 0; F < 8; ++F) FA[F] = vr(F >> 2, F & 3);
#pragma unroll
      for (int F = 0; F < 8; ++F) FB[F] = vr(2 + (F >> 2), F & 3);
      SBAR();
      auto store_tile = [&](int T) {
#pragma unroll
        for (int s = 0; s < 2; ++s) {
          u32x4 w = {pk2(acc[T][8 * s + 0] * inv, acc[T][8 * s + 1] * inv), pk2(acc[T][8 * s + 2] * inv, acc[T][8 * s + 3] * inv),
                     pk2(acc[T][8 * s + 4] * inv, acc[T][8 * s + 5] * inv), pk2(acc[T][8 * s + 6] * inv, acc[T][8 * s + 7] * inv)};
          OSTORE((u32x4*)(op + T * 1024 + s * 256), w);
        }
      };
#pragma unroll
      for (int u = 0; u < 4; ++u) acc[0] = mfma32(FA[u], asv(pf[u]), acc[0]);
#pragma unroll
      for (int u = 0; u < 4; ++u) acc[1] = mfma32(FA[4 + u], asv(pf[u]), acc[1]);
      SBAR();
#pragma unroll
      for (int u = 0; u < 4; ++u) acc[2] = mfma32(FB[u], asv(pf[u]), acc[2]);
      store_tile(0);
      SBAR();
#pragma unroll
      for (int u = 0; u < 4; ++u) acc[3] = mfma32(FB[4 + u], asv(pf[u]), acc[3]);
      store_tile(1);
      SBAR();
      barrier_nw();
      store_tile(2);
      store_tile(3);
    }
  }
}

constexpr int K2_STAGE = 7168;
constexpr int K2_WAVE = 2 * K2_STAGE;
constexpr int K2_LDS = 8 * K2_WAVE;
__global__ __launch_bounds__(512) void k2_proj(const char* __restrict__ ows, const char* __restrict__ wo, char* __restrict__ out) {
  __shared__ __attribute__((aligned(256))) char smem[K2_LDS];
  const int lane = threadIdx.x & 63;
  const int wid = __builtin_amdgcn_readfirstlane(threadIdx.x >> 6);
  const int xcd = blockIdx.x & 7, idx = blockIdx.x >> 3;
  const int ig = 2 * (xcd & 1) + ((xcd >> 1) & 1), jg = xcd >> 2;
  const int nn = jg ? 4 : 5;
  if (idx >= 6 * nn) return;
  const int mtile = 6 * ig + idx / nn, ntile = (jg ? 5 : 0) + idx % nn;
  const unsigned lds0 = (unsigned)(size_t)(LDSP char*)smem;
  const unsigned lw = lds0 + wid * K2_WAVE;
  const char* ag = ows + ((size_t)(3 * mtile) * 128 + wid) * 1024;
  const char* bg = wo + (size_t)(32 * wid) * (DM * 2) + ntile * 128;
  const unsigned aoff = lane * 16;
  unsigned boff[4];
#pragma unroll
  for (int n = 0; n < 4; ++n) {
    const int row = 8 * n + (lane >> 3), f = ((row >> 1) & 1) | (((row >> 4) & 1) << 1);
    boff[n] = row * (DM * 2) + (((lane & 7) ^ (f << 1)) << 4);
  }
  auto issue = [&](int i, auto BC) {
    constexpr int Bf = decltype(BC)::value * K2_STAGE;
    const char* a = ag + (size_t)i * 8192;
    const char* bp = bg + (size_t)i * (256 * DM * 2);
    dma16i<K2A_NT, Bf>(a, aoff, lw);
    dma16i<K2A_NT, Bf + 1024>(a + 131072, aoff, lw);
    dma16i<K2A_NT, Bf + 2048>(a + 262144, aoff, lw);
    dma16i<K2B_NT, Bf + 3072>(bp, boff[0], lw);
    dma16i<K2B_NT, Bf + 4096>(bp, boff[1], lw);
    dma16i<K2B_NT, Bf + 5120>(bp, boff[2], lw);
    dma16i<K2B_NT, Bf + 6144>(bp, boff[3], lw);
  };
  issue(0, IC(0));
  issue(1, IC(1));
  const int g = lane >> 4, qp = (lane >> 2) & 3, p = lane & 3;
  unsigned bad[4];
  {
    const int f = ((qp >> 1) & 1) | ((g & 1) << 1);
#pragma unroll
    for (int t = 0; t < 4; ++t) bad[t] = lw + 3072 + (16 * (g & 1) + 4 * (g >> 1) + qp) * 128 + 32 * (t ^ f) + 8 * p;
  }
  const unsigned aad = lw + lane * 16;
  f32x4 acc[3][4];
#pragma unroll
  for (int m = 0; m < 3; ++m)
#pragma unroll
    for (int t = 0; t < 4; ++t) acc[m][t] = f32x4{0.f, 0.f, 0.f, 0.f};
  auto step = [&](int i, auto BC, auto LASTC) {
    constexpr int Bf = decltype(BC)::value * K2_STAGE;
    constexpr int MODE = decltype(LASTC)::value;
    if constexpr (MODE == 2) wait_vm<0>(); else wait_vm<7>();
    s16x8 af[3], bf[4];
#pragma unroll
    for (int m = 0; m < 3; ++m) af[m] = ldsread16(aad + Bf + m * 1024);
#pragma unroll
    for (int t = 0; t < 4; ++t) bf[t] = cat(trread(bad[t] + Bf), trread(bad[t] + Bf + 1024));
    asm volatile("s_waitcnt lgkmcnt(0)" ::: "memory");
    SBAR();
    if constexpr (MODE == 0) issue(i + 2, BC);
    SBAR();
#pragma unroll
    for (int m = 0; m < 3; ++m)
#pragma unroll
      for (int t = 0; t < 4; ++t) acc[m][t] = mfma16(bf[t], af[m], acc[m][t]);
    SBAR();
  };
#pragma unroll 1
  for (int i = 0; i < 14; i += 2) {
    step(i, IC(0), IC(0));
    step(i + 1, IC(1), IC(0));
  }
  step(14, IC(0), IC(1));
  step(15, IC(1), IC(2));
  barrier();
#pragma unroll
  for (int m = 0; m < 3; ++m)
#pragma unroll
    for (int t = 0; t < 4; ++t) *(LDSP f32x4*)(size_t)(lds0 + (wid * 12 + m * 4 + t) * 1024 + lane * 16) = acc[m][t];
  barrier();
#pragma unroll
  for (int rep = 0; rep < 2; ++rep) {
    const int u = rep * 8 + wid;
    if (u < 12) {
      f32x4 sum = *(LDSP f32x4*)(size_t)(lds0 + u * 1024 + lane * 16);
#pragma unroll
      for (int w = 1; w < 8; ++w) sum += *(LDSP f32x4*)(size_t)(lds0 + (w * 12 + u) * 1024 + lane * 16);
      const int m = u >> 2, t = u & 3;
      u32x2 v = {pk2(sum[0], sum[1]), pk2(sum[2], sum[3])};
      *(u32x2*)(out + ((size_t)(mtile * 48 + m * 16 + (lane & 15)) * DM + ntile * 64 + t * 16 + 4 * g) * 2) = v;
    }
  }
}

}

extern "C" void kernel_launch(void* const* d_in, const int* in_sizes, int n_in, void* d_out, int out_size, void* d_ws, size_t ws_size,
                              hipStream_t stream) {
  const char* x = (const char*)d_in[0];
  const char* kc = (const char*)d_in[1];
  const char* vc = (const char*)d_in[2];
  const char* wq = (const char*)d_in[3];
  const char* wo = (const char*)d_in[4];
  char* ows = (char*)d_ws;
  k1_attn<<<256, 512, 0, stream>>>(x, kc, vc, wq, wo, ows);
  k2_proj<<<256, 512, 0, stream>>>(ows, wo, (char*)d_out);
}
```

```cpp
#include <hip/hip_runtime.h>
#include <hip/hip_bf16.h>
#include <utility>

namespace {

constexpr int NB = 8, SQ = 144, DM = 576, NH = 32, SK = 512, DH = 128;

typedef __attribute__((ext_vector_type(4))) short s16x4;
typedef __attribute__((ext_vector_type(8))) short s16x8;
typedef __attribute__((ext_vector_type(4))) float f32x4;
typedef __attribute__((ext_vector_type(16))) float f32x16;
typedef __attribute__((ext_vector_type(8))) __bf16 bf16x8;
typedef __attribute__((ext_vector_type(2))) __bf16 bf16x2;
typedef __attribute__((ext_vector_type(2))) float f32x2;
typedef __attribute__((ext_vector_type(4))) unsigned u32x4;
typedef __attribute__((ext_vector_type(2))) unsigned u32x2;
#define LDSP __attribute__((address_space(3)))
#define DI __device__ __forceinline__

DI f32x4 mfma16(s16x8 a, s16x8 b, f32x4 c) {
  return __builtin_amdgcn_mfma_f32_16x16x32_bf16(__builtin_bit_cast(bf16x8, a), __builtin_bit_cast(bf16x8, b), c, 0, 0, 0);
}
DI f32x4 mfma16k16(s16x4 a, s16x4 b, f32x4 c) { return __builtin_amdgcn_mfma_f32_16x16x16bf16_1k(a, b, c, 0, 0, 0); }
DI f32x16 mfma32(s16x8 a, s16x8 b, f32x16 c) {
  return __builtin_amdgcn_mfma_f32_32x32x16_bf16(__builtin_bit_cast(bf16x8, a), __builtin_bit_cast(bf16x8, b), c, 0, 0, 0);
}
DI unsigned pk2(float lo, float hi) {
  f32x2 v = {lo, hi};
  bf16x2 r = __builtin_convertvector(v, bf16x2);
  return __builtin_bit_cast(unsigned, r);
}
DI s16x4 trread(unsigned addr) { return __builtin_amdgcn_ds_read_tr16_b64_v4i16((LDSP s16x4*)(size_t)addr); }
DI s16x8 ldsread16(unsigned addr) { return *(LDSP s16x8*)(size_t)addr; }
DI s16x8 cat(s16x4 a, s16x4 b) { return __builtin_shufflevector(a, b, 0, 1, 2, 3, 4, 5, 6, 7); }
DI float max3(float a, float b, float c) { return __builtin_fmaxf(__builtin_fmaxf(a, b), c); }
DI float xmax32(float v) {
  auto r = __builtin_amdgcn_permlane32_swap(__float_as_uint(v), __float_as_uint(v), false, false);
  return fmaxf(__uint_as_float(r[0]), __uint_as_float(r[1]));
}
DI float xmax16(float v) {
  auto r = __builtin_amdgcn_permlane16_swap(__float_as_uint(v), __float_as_uint(v), false, false);
  return fmaxf(__uint_as_float(r[0]), __uint_as_float(r[1]));
}
DI float xadd32(float v) {
  auto r = __builtin_amdgcn_permlane32_swap(__float_as_uint(v), __float_as_uint(v), false, false);
  return __uint_as_float(r[0]) + __uint_as_float(r[1]);
}
DI float xadd16(float v) {
  auto r = __builtin_amdgcn_permlane16_swap(__float_as_uint(v), __float_as_uint(v), false, false);
  return __uint_as_float(r[0]) + __uint_as_float(r[1]);
}

template <bool NT>
DI void dma16(const char* sbase, unsigned voff, unsigned lds) {
  if constexpr (NT)
    asm volatile("s_mov_b32 m0, %1\n\ts_nop 0\n\tglobal_load_lds_dwordx4 %0, %2 nt" ::"v"(voff), "s"(lds), "s"(sbase) : "memory");
  else
    asm volatile("s_mov_b32 m0, %1\n\ts_nop 0\n\tglobal_load_lds_dwordx4 %0, %2" ::"v"(voff), "s"(lds), "s"(sbase) : "memory");
}
DI void dma16_lo(const char* sbase, unsigned voff, unsigned lds) {
  asm volatile("s_mov_b32 m0, %1\n\ts_mov_b32 exec_hi, 0\n\tglobal_load_lds_dwordx4 %0, %2\n\ts_mov_b32 exec_hi, -1" ::"v"(voff), "s"(lds), "s"(sbase) : "memory");
}
template <bool NT, int IMM>
DI void dma16i(const char* sbase, unsigned voff, unsigned lds) {
  if constexpr (NT)
    asm volatile("s_add_u32 m0, %1, %3\n\ts_nop 0\n\tglobal_load_lds_dwordx4 %0, %2 nt" ::"v"(voff), "s"(lds), "s"(sbase), "i"(IMM) : "memory", "scc");
  else
    asm volatile("s_add_u32 m0, %1, %3\n\ts_nop 0\n\tglobal_load_lds_dwordx4 %0, %2" ::"v"(voff), "s"(lds), "s"(sbase), "i"(IMM) : "memory", "scc");
}
template <int IMM>
DI void dma16i_lo(const char* sbase, unsigned voff, unsigned lds) {
  asm volatile("s_add_u32 m0, %1, %3\n\ts_mov_b32 exec_hi, 0\n\tglobal_load_lds_dwordx4 %0, %2\n\ts_mov_b32 exec_hi, -1" ::"v"(voff), "s"(lds), "s"(sbase), "i"(IMM) : "memory", "scc");
}
template <int N> DI void wait_vm() { asm volatile("s_waitcnt vmcnt(%0)" ::"i"(N) : "memory"); }
#define STAMP(i)
DI void barrier() { asm volatile("s_waitcnt lgkmcnt(0)\n\ts_barrier" ::: "memory"); }
DI void barrier_nw() { asm volatile("s_barrier" ::: "memory"); }
template <int... I, class F> DI void for_seq(std::integer_sequence<int, I...>, F&& f) { (f(std::integral_constant<int, I>{}), ...); }
#define IC(n) std::integral_constant<int, n>{}
#define SGB(mask, n) __builtin_amdgcn_sched_group_barrier(mask, n, 0)
#define SBAR() __builtin_amdgcn_sched_barrier(0)

#define KV_NT true
#define LOOKAHEAD 4
#define OSTORE(p, v) (*(p) = (v))
#define K2A_NT false
#define K2B_NT false
#define Q_NT false
#define HPRIO 2
#define EARLYW -1

constexpr int SLOT = 17408;
constexpr int GRP = 1088;
constexpr int QRING = 3;
constexpr int QPOS0 = 3 * SLOT;
constexpr int QX_OFF = 9 * SLOT;
constexpr int ST_OFF = QX_OFF + 4096;
constexpr int SY_OFF = ST_OFF + 512;
constexpr int RDYQ = SY_OFF, RDYT = SY_OFF + 16, DONEC = SY_OFF + 32, DONEH = SY_OFF + 48, AUXF = SY_OFF + 64;
constexpr int DUMMY_OFF = SY_OFF + 128;
constexpr int STAMP_OFF = DUMMY_OFF + 1024;
constexpr int K1_LDS = STAMP_OFF + 512;
constexpr int CMB_OFF = 7 * SLOT;
constexpr float CSC = 0.08838834764831845f * 1.4426950408889634f;
constexpr float THR = 8.0f;

constexpr int NEV = 19;
constexpr int need_ev(int it) {
  if (it < 9) return it;
  int a = it - 9;
  return a < 2 ? 9 + a : (a == 15 ? 18 : 11 + (a - 2) / 2);
}
constexpr int free_ev(int it) { return it < 9 ? it + 1 : need_ev(it) + 2; }
struct Sched { int n; int item[48]; int when[48]; };
#define WOPF_EV 16
#define LA2 4
#define EARLYT 0
constexpr Sched make_sched() {
  Sched S{};
  int n = 0;
  auto add = [&](int it, int w) { S.item[n] = it; S.when[n] = w; ++n; };
  add(0, -1); add(1, -1);
  for (int a = 0; a < EARLYT; ++a) add(9 + a, EARLYW);
  int nt = EARLYT;
  for (int e = 0; e < NEV; ++e) {
    if (e <= 6) add(e + 2, e);
    int lim = (e == 7) ? 2 : (e == 8 ? 4 : (e == 9 ? LA2 + 1 : (e == 10 ? LA2 + 2 : (e >= 11 ? 2 * (e - 11) + 4 + LA2 : 0))));
    if (lim > 16) lim = 16;
    for (; nt < lim; ++nt) add(9 + nt, e);
    if (e == WOPF_EV) for (int k = 0; k < 5; ++k) add(100 + k, e);
  }
  for (int i = 1; i < n; ++i)
    for (int j = i; j > 0 && S.when[j - 1] > S.when[j]; --j) {
      int t = S.when[j]; S.when[j] = S.when[j - 1]; S.when[j - 1] = t;
      t = S.item[j]; S.item[j] = S.item[j - 1]; S.item[j - 1] = t;
    }
  S.n = n;
  return S;
}
constexpr Sched SCH = make_sched();
constexpr int item_ops(int it) { return it < 9 ? 9 : (it < 100 ? 4 : 1); }
constexpr int sched_wait(int ev) {
  int pos = -1, cnt = 0;
  for (int e = 0; e < SCH.n; ++e) if (SCH.item[e] < 100 && need_ev(SCH.item[e]) == ev) pos = e;
  for (int e = pos + 1; e < SCH.n; ++e) if (SCH.when[e] < ev) cnt += item_ops(SCH.item[e]);
  return cnt;
}
constexpr bool sched_ok() {
  for (int e = 0; e < SCH.n; ++e) {
    int it = SCH.item[e], w = SCH.when[e];
    if (it >= 100) continue;
    if (w >= need_ev(it)) return false;
    int prev = -1;
    if (it < 9) { if (it >= QRING) prev = it - QRING; }
    else { int a = it - 9; if (a >= 9) prev = it - 9; else if (a >= 3) prev = 6 + (a - 3) / 2; }
    if (prev >= 0 && w < free_ev(prev)) return false;
  }
  for (int ev = 0; ev < NEV; ++ev) if (sched_wait(ev) > 60) return false;
  return true;
}
static_assert(sched_ok(), "loader schedule violates a slot or ordering constraint");

__global__ __launch_bounds__(512) void k1_attn(const char* __restrict__ x, const char* __restrict__ kc, const char* __restrict__ vc,
                                               const char* __restrict__ wq, const char* __restrict__ wo, char* __restrict__ ows) {
  __shared__ __attribute__((aligned(256))) char smem[K1_LDS];
  const int tid = threadIdx.x;
  const int lane = tid & 63;
  const int wid = __builtin_amdgcn_readfirstlane(tid >> 6);
  const int xcd = blockIdx.x & 7, idx = blockIdx.x >> 3;
  const int b = 4 * (xcd & 1) + (idx & 3);
  const int h = 8 * (xcd >> 1) + (idx >> 2);
  const unsigned lds0 = (unsigned)(size_t)(LDSP char*)smem;
  STAMP(0);

  if (wid >= 4) {
    const int hi = wid - 4;
    const char* xg = x + (size_t)(b * SQ) * (DM * 2);
    const char* wg = wq + h * 256;
    const char* kg = kc + (size_t)(b * NH + h) * (SK * DH * 2);
    const char* vg = vc + (size_t)(b * NH + h) * (SK * DH * 2);
    unsigned xoff[4], xoffe, woff[4], koff[4], voff[4];
    {
      const int cc = lane & 7, c16 = lane & 15;
#pragma unroll
      for (int n = 0; n < 4; ++n) {
        int rx = hi * 32 + n * 8 + (lane >> 3);
        xoff[n] = rx * (DM * 2) + ((cc ^ ((rx >> 1) & 7)) << 4);
        int r = hi * 16 + n * 4 + (lane >> 4);
        woff[n] = r * 8192 + ((c16 ^ (((r & 3) << 2) | (((r >> 3) & 1) << 1))) << 4);
        koff[n] = r * 256 + ((c16 ^ (r & 3)) << 4);
        voff[n] = r * 256 + (c16 << 4);
      }
      int rx = 128 + hi * 4 + ((lane >> 3) & 3);
      xoffe = rx * (DM * 2) + ((cc ^ ((rx >> 1) & 7)) << 4);
    }
    const unsigned lq = lds0 + QPOS0 + hi * 4096;
    const unsigned lqe = lds0 + QPOS0 + 32768 + hi * 512;
    const unsigned lt = lds0 + hi * (4 * GRP);
    auto issue_q = [&](auto SC) {
      constexpr int S = decltype(SC)::value;
      constexpr int P = (S % 3) * (2 * SLOT);
      const char* ws = wg + (size_t)S * (64 * 8192);
      const char* xs = xg + S * 128;
      dma16i<Q_NT, P>(ws, woff[0], lq); dma16i<Q_NT, P + 1024>(ws, woff[1], lq);
      dma16i<Q_NT, P + 2048>(ws, woff[2], lq); dma16i<Q_NT, P + 3072>(ws, woff[3], lq);
      dma16i<Q_NT, P + 16384>(xs, xoff[0], lq); dma16i<Q_NT, P + 16384 + 1024>(xs, xoff[1], lq);
      dma16i<Q_NT, P + 16384 + 2048>(xs, xoff[2], lq); dma16i<Q_NT, P + 16384 + 3072>(xs, xoff[3], lq);
      dma16i_lo<P>(xs, xoffe, lqe);
    };
    auto issue_t = [&](auto AC) {
      constexpr int A = decltype(AC)::value;
      constexpr int L = (A % 9) * SLOT;
      constexpr bool isK = (A == 0) || ((A & 1) && A != 15);
      if constexpr (isK) {
        const char* sp = kg + ((A + 1) >> 1) * 16384;
        dma16i<KV_NT, L>(sp, koff[0], lt); dma16i<KV_NT, L + GRP>(sp, koff[1], lt);
        dma16i<KV_NT, L + 2 * GRP>(sp, koff[2], lt); dma16i<KV_NT, L + 3 * GRP>(sp, koff[3], lt);
      } else {
        const char* sp = vg + ((A == 15) ? 7 : ((A - 2) >> 1)) * 16384;
        dma16i<KV_NT, L>(sp, voff[0], lt); dma16i<KV_NT, L + GRP>(sp, voff[1], lt);
        dma16i<KV_NT, L + 2 * GRP>(sp, voff[2], lt); dma16i<KV_NT, L + 3 * GRP>(sp, voff[3], lt);
      }
    };
    const char* wop = wo + (size_t)blockIdx.x * 18432 + hi * 4608;
    auto issue_at = [&](auto CC) {
      constexpr int C = decltype(CC)::value;
      for_seq(std::make_integer_sequence<int, SCH.n>{}, [&](auto EC) {
        constexpr int E = decltype(EC)::value;
        if constexpr (SCH.when[E] == C) {
          constexpr int IT = SCH.item[E];
          if constexpr (IT < 9) issue_q(IC(IT));
          else if constexpr (IT < 100) issue_t(IC(IT - 9));
          else if constexpr (IT < 104) dma16i<false, DUMMY_OFF>(wop + (IT - 100) * 1024, lane * 16, lds0);
          else dma16i_lo<DUMMY_OFF>(wop + 4096, lane * 16, lds0);
        }
      });
    };
    issue_at(IC(-1));

    const int r16 = lane & 15, g = lane >> 4, qp = (lane >> 2) & 3, p = lane & 3;
    unsigned wbase[2], xbase[2], kb, vb;
    {
      const int ew = (qp << 1) | (g & 1);
#pragma unroll
      for (int t = 0; t < 2; ++t) wbase[t] = lds0 + QPOS0 + (8 * g + qp) * 256 + 32 * ((2 * hi + t) ^ ew) + 8 * p;
      const int rx = 128 + r16;
#pragma unroll
      for (int s = 0; s < 2; ++s) xbase[s] = lds0 + QPOS0 + 16384 + rx * 128 + (((4 * s + g) ^ ((rx >> 1) & 7)) << 4);
      kb = lds0 + (4 * hi + (r16 & 3)) * GRP + (r16 >> 2) * 256 + ((g ^ (r16 >> 2)) << 4);
      vb = lds0 + (4 * hi + qp) * GRP + g * 256 + 8 * p;
    }
    __builtin_amdgcn_s_setprio(HPRIO);
    f32x4 qa[2] = {f32x4{0.f, 0.f, 0.f, 0.f}, f32x4{0.f, 0.f, 0.f, 0.f}};
    for_seq(std::make_integer_sequence<int, 9>{}, [&](auto SC) {
      constexpr int S = decltype(SC)::value;
      wait_vm<sched_wait(S)>();
      if constexpr (S == 0) STAMP(1);
      barrier();
      constexpr unsigned P = (S % 3) * (2 * SLOT);
      s16x8 xb[2], wf[2][2];
#pragma unroll
      for (int ks = 0; ks < 2; ++ks) {
        xb[ks] = ldsread16(xbase[ks] + P);
#pragma unroll
        for (int t = 0; t < 2; ++t) wf[ks][t] = cat(trread(wbase[t] + P + ks * 8192), trread(wbase[t] + P + ks * 8192 + 1024));
      }
      SBAR();
      issue_at(SC);
      SBAR();
#pragma unroll
      for (int ks = 0; ks < 2; ++ks)
#pragma unroll
        for (int t = 0; t < 2; ++t) {
          qa[t] = mfma16(wf[ks][t], xb[ks], qa[t]);
        }
    });
    {
      qa[0] *= CSC; qa[1] *= CSC;
      unsigned a0 = pk2(qa[0][0], qa[0][1]), a1 = pk2(qa[0][2], qa[0][3]);
      unsigned b0 = pk2(qa[1][0], qa[1][1]), b1 = pk2(qa[1][2], qa[1][3]);
      auto r0 = __builtin_amdgcn_permlane32_swap(a0, b0, false, false);
      auto r1 = __builtin_amdgcn_permlane32_swap(a1, b1, false, false);
      auto t0 = __builtin_amdgcn_permlane16_swap(r0[0], r0[1], false, false);
      auto t1 = __builtin_amdgcn_permlane16_swap(r1[0], r1[1], false, false);
      u32x4 w = {t0[0], t1[0], t0[1], t1[1]};
      *(LDSP u32x4*)(size_t)(lds0 + QX_OFF + hi * 1024 + lane * 16) = w;
    }
    STAMP(2);
    s16x8 qf[4];
    float m_run = -__builtin_inff(), l_run = 0.f, al_pend = 1.f;
    f32x4 oa[8];
#pragma unroll
    for (int T = 0; T < 8; ++T) oa[T] = f32x4{0.f, 0.f, 0.f, 0.f};
    s16x4 p_cur = {0, 0, 0, 0}, p_nxt = {0, 0, 0, 0};
    bool hpend = false;
    auto kstep = [&](const s16x8 (&kf)[4], bool first) {
      f32x4 s = {0.f, 0.f, 0.f, 0.f};
#pragma unroll
      for (int ks = 0; ks < 4; ++ks) s = mfma16(kf[ks], qf[ks], s);
      float mx = max3(fmaxf(s[0], s[1]), s[2], s[3]);
      mx = xmax32(xmax16(mx));
      if (first) {
        m_run = mx;
      } else if (__builtin_expect(__any(mx > m_run + THR), 0)) {
        float m_new = fmaxf(m_run, mx);
        al_pend = __builtin_amdgcn_exp2f(m_run - m_new);
        m_run = m_new;
        l_run *= al_pend;
        hpend = true;
      }
#pragma unroll
      for (int r = 0; r < 4; ++r) { s[r] = __builtin_amdgcn_exp2f(s[r] - m_run); l_run += s[r]; }
      u32x2 w = {pk2(s[0], s[1]), pk2(s[2], s[3])};
      p_nxt = __builtin_bit_cast(s16x4, w);
      if (first) p_cur = p_nxt;
    };
    auto vstep = [&](const s16x4 (&vf)[8], bool last) {
#pragma unroll
      for (int T = 0; T < 8; ++T) oa[T] = mfma16k16(vf[T], p_cur, oa[T]);
      if (!last && __builtin_expect(hpend, 0)) {
#pragma unroll
        for (int T = 0; T < 8; ++T) oa[T] *= al_pend;
        hpend = false;
      }
      p_cur = p_nxt;
    };
    for_seq(std::make_integer_sequence<int, 10>{}, [&](auto EC) {
      constexpr int EV = 9 + decltype(EC)::value;
      wait_vm<sched_wait(EV)>();
      if constexpr (EV == 9) STAMP(3);
      if constexpr (EV == 13) STAMP(4);
      if constexpr (EV == 18) STAMP(5);
      barrier();
      constexpr int AV = (EV >= 11) ? (EV == 18 ? 15 : 2 * (EV - 11) + 2) : -1;
      constexpr int AK = (EV == 9) ? 0 : (EV == 10 ? 1 : (EV <= 16 ? 2 * (EV - 11) + 3 : -1));
      if constexpr (EV == 9) {
#pragma unroll
        for (int s = 0; s < 4; ++s) qf[s] = ldsread16(lds0 + QX_OFF + s * 1024 + lane * 16);
      }
      s16x8 kf[4]; s16x4 vf[8];
      if constexpr (AV >= 0) {
#pragma unroll
        for (int T = 0; T < 8; ++T) vf[T] = trread(vb + (AV % 9) * SLOT + T * 32);
      }
      if constexpr (AK >= 0) {
#pragma unroll
        for (int ks = 0; ks < 4; ++ks) kf[ks] = ldsread16(kb + (AK % 9) * SLOT + ks * 64);
      }
      SBAR();
      issue_at(IC(EV));
      SBAR();
      if constexpr (AV >= 0) vstep(vf, AV == 15);
      if constexpr (AK >= 0) kstep(kf, AK == 0);
    });
    {
      float l = xadd32(xadd16(l_run));
      if (g == 0) {
        f32x2 st = {m_run, l};
        *(LDSP f32x2*)(size_t)(lds0 + ST_OFF + (hi * 16 + r16) * 8) = st;
      }
#pragma unroll
      for (int T = 0; T < 8; ++T) *(LDSP f32x4*)(size_t)(lds0 + CMB_OFF + (hi * 8 + T) * 1024 + lane * 16) = oa[T];
    }
    barrier();
    {
      f32x2 st[4];
#pragma unroll
      for (int k = 0; k < 4; ++k) st[k] = *(LDSP f32x2*)(size_t)(lds0 + ST_OFF + (k * 16 + r16) * 8);
      float m = fmaxf(fmaxf(st[0][0], st[1][0]), fmaxf(st[2][0], st[3][0]));
      float sc[4], l = 0.f;
#pragma unroll
      for (int k = 0; k < 4; ++k) { sc[k] = __builtin_amdgcn_exp2f(st[k][0] - m); l += sc[k] * st[k][1]; }
      float inv = 1.0f / l;
      const int T = 2 * hi + (g & 1);
      f32x4 lo = {0.f, 0.f, 0.f, 0.f}, hi4 = {0.f, 0.f, 0.f, 0.f};
#pragma unroll
      for (int k = 0; k < 4; ++k) {
        unsigned base = lds0 + CMB_OFF + (k * 8 + T) * 1024 + r16 * 16;
        f32x4 a = *(LDSP f32x4*)(size_t)(base + (g >> 1) * 256);
        f32x4 c2 = *(LDSP f32x4*)(size_t)(base + (2 + (g >> 1)) * 256);
        lo += a * sc[k];
        hi4 += c2 * sc[k];
      }
      u32x4 w = {pk2(lo[0] * inv, lo[1] * inv), pk2(lo[2] * inv, lo[3] * inv), pk2(hi4[0] * inv, hi4[1] * inv), pk2(hi4[2] * inv, hi4[3] * inv)};
      OSTORE((u32x4*)(ows + ((size_t)(b * 9 + 8) * 128 + h * 4 + hi) * 1024 + lane * 16), w);
    }
  } else {
    const int r32 = lane & 31, hh = lane >> 5, cb = (lane >> 4) & 1, qp = (lane >> 2) & 3, p = lane & 3;
    f32x16 acc[4];
#pragma unroll
    for (int T = 0; T < 4; ++T)
#pragma unroll
      for (int r = 0; r < 16; ++r) acc[T][r] = 0.f;
    s16x8 FA[8], FB[8];
    using T_ = std::true_type; using F_ = std::false_type;

    {
      unsigned wad[4], xad[4];
#pragma unroll
      for (int T = 0; T < 4; ++T) {
        unsigned ch = ((4 * T + 2 * cb + (p >> 1)) ^ ((qp << 2) | (hh << 1)));
        wad[T] = lds0 + QPOS0 + (8 * hh + qp) * 256 + ch * 16 + 8 * (p & 1);
      }
      const int rx = wid * 32 + r32;
#pragma unroll
      for (int ks = 0; ks < 4; ++ks) xad[ks] = lds0 + QPOS0 + 16384 + rx * 128 + (((2 * ks + hh) ^ ((rx >> 1) & 7)) << 4);
      s16x8 xa[2], xb[2];
      auto qhalf = [&](s16x8 (&CUR)[8], s16x8 (&xc)[2], s16x8 (&NXT)[8], s16x8 (&xn)[2], auto HC, auto DOMM, auto DOLD) {
        constexpr int HALF = decltype(HC)::value;
        auto rdw = [&](int F) {
          const int k = F >> 2, T = F & 3;
          NXT[F] = cat(trread(wad[T] + (2 * HALF + k) * 4096), trread(wad[T] + (2 * HALF + k) * 4096 + 1024));
        };
        for_seq(std::make_integer_sequence<int, 8>{}, [&](auto IC_) {
          constexpr int I = decltype(IC_)::value;
          if constexpr (decltype(DOMM)::value) { acc[I & 3] = mfma32(CUR[I], xc[I >> 2], acc[I & 3]); SBAR(); }
          if constexpr (decltype(DOLD)::value) {
            if constexpr (I == 0) { xn[0] = ldsread16(xad[2 * HALF]); rdw(0); }
            if constexpr (I == 1) { rdw(1); rdw(2); }
            if constexpr (I == 2) { rdw(3); xn[1] = ldsread16(xad[2 * HALF + 1]); }
            if constexpr (I == 3) { rdw(4); rdw(5); }
            if constexpr (I == 4) { rdw(6); }
            if constexpr (I == 5) { rdw(7); }
          }
          SBAR();
        });
      };
      barrier();
      STAMP(1);
      qhalf(FB, xb, FA, xa, IC(0), F_{}, T_{});
      qhalf(FA, xa, FB, xb, IC(1), T_{}, T_{});
      int pos = 0;
#pragma unroll 1
      for (int S = 0; S < 8; ++S) {
        const unsigned d = (pos == QRING - 1) ? (unsigned)(-2 * (QRING - 1) * SLOT) : (unsigned)(2 * SLOT);
        pos = (pos == QRING - 1) ? 0 : pos + 1;
#pragma unroll
        for (int T = 0; T < 4; ++T) { wad[T] += d; xad[T] += d; }
        asm volatile("" : "+v"(wad[0]), "+v"(wad[1]), "+v"(wad[2]), "+v"(wad[3]), "+v"(xad[0]), "+v"(xad[1]), "+v"(xad[2]), "+v"(xad[3]));
        barrier();
        SBAR();
        qhalf(FB, xb, FA, xa, IC(0), T_{}, T_{});
        qhalf(FA, xa, FB, xb, IC(1), T_{}, T_{});
      }
      qhalf(FB, xb, FA, xa, IC(0), T_{}, F_{});
    }
    STAMP(2);

    s16x8 qf[8];
#pragma unroll
    for (int T = 0; T < 4; ++T) {
      acc[T] *= CSC;
#pragma unroll
      for (int s = 0; s < 2; ++s) {
        unsigned a0 = pk2(acc[T][8 * s + 0], acc[T][8 * s + 1]), a1 = pk2(acc[T][8 * s + 2], acc[T][8 * s + 3]);
        unsigned b0 = pk2(acc[T][8 * s + 4], acc[T][8 * s + 5]), b1 = pk2(acc[T][8 * s + 6], acc[T][8 * s + 7]);
        auto r0 = __builtin_amdgcn_permlane32_swap(a0, b0, false, false);
        auto r1 = __builtin_amdgcn_permlane32_swap(a1, b1, false, false);
        u32x4 w = {r0[0], r1[0], r0[1], r1[1]};
        qf[2 * T + s] = __builtin_bit_cast(s16x8, w);
      }
    }
#pragma unroll
    for (int T = 0; T < 4; ++T)
#pragma unroll
      for (int r = 0; r < 16; ++r) acc[T][r] = 0.f;

    unsigned kad[2], vad;
    {
      const int a_ = r32 >> 3, h_ = (r32 >> 2) & 1, j_ = r32 & 3;
      const int grp = 4 * (a_ >> 1) + j_, rin = 2 * (a_ & 1) + h_;
#pragma unroll
      for (int e = 0; e < 2; ++e) kad[e] = lds0 + grp * GRP + rin * 256 + (((2 * e + hh) ^ rin) << 4);
      vad = lds0 + qp * GRP + hh * 256 + 32 * cb + 8 * p;
    }
    f32x16 s0, s1, negm;
    u32x4 pf[4];
    float l_run = 0.f, al_pend = 1.f, mx = 0.f, ps0 = 0.f, ps1 = 0.f;
    bool pend = false;
#define PIN1(a) asm volatile("" : "+v"(a))
#define PIN2(a, b) asm volatile("" : "+v"(a), "+v"(b))
#define PIN4(a, b, c, d) asm volatile("" : "+v"(a), "+v"(b), "+v"(c), "+v"(d))
    auto kread = [&](int F, int t) { return ldsread16(kad[F & 1] + t * (8 * GRP) + (F >> 1) * 64); };
    auto vread = [&](int F, int hf) {
      const unsigned o = (8 * hf + 4 * (F >> 2)) * GRP + 64 * (F & 3);
      return cat(trread(vad + o), trread(vad + o + 512));
    };
    auto EXP2 = [&](f32x16& s, int r) {
      float e0 = __builtin_amdgcn_exp2f(s[r]), e1 = __builtin_amdgcn_exp2f(s[r + 1]);
      PIN2(e0, e1);
      s[r] = e0; s[r + 1] = e1;
    };
    auto ADD2 = [&](const f32x16& s, int q) {
      ps0 += s[q]; ps1 += s[q + 1]; PIN2(ps0, ps1); };
    auto CVT2 = [&](const f32x16& s, int i, u32x4& lo, u32x4& hi2) {
      unsigned w0 = pk2(s[2 * i], s[2 * i + 1]), w1 = pk2(s[2 * i + 2], s[2 * i + 3]);
      PIN2(w0, w1);
      if (i < 4) { lo[i] = w0; lo[i + 1] = w1; } else { hi2[i - 4] = w0; hi2[i - 3] = w1; }
    };
    auto asv = [](const u32x4& v) { return __builtin_bit_cast(s16x8, v); };
    auto fin1 = [&](auto IC_) {
      constexpr int I = decltype(IC_)::value;
      if constexpr (I < 7) ADD2(s1, 2 + 2 * I);
      if constexpr (I < 6) EXP2(s1, 4 + 2 * I);
      if constexpr (I == 6) { CVT2(s1, 0, pf[2], pf[3]); CVT2(s1, 2, pf[2], pf[3]); }
      if constexpr (I == 7) { CVT2(s1, 4, pf[2], pf[3]); CVT2(s1, 6, pf[2], pf[3]); }
    };

    auto H1 = [&](auto FIN) {
      for_seq(std::make_integer_sequence<int, 8>{}, [&](auto IC_) {
        constexpr int I = decltype(IC_)::value;
        if constexpr (I == 0) s0 = mfma32(FA[0], qf[0], negm); else s0 = mfma32(FA[I], qf[I], s0);
        SBAR();
        FB[I] = kread(I, 1);
        if constexpr (decltype(FIN)::value) fin1(IC_);
        SBAR();
      });
    };
    auto H2 = [&](auto RDV) {
      for_seq(std::make_integer_sequence<int, 8>{}, [&](auto IC_) {
        constexpr int I = decltype(IC_)::value;
        if constexpr (I == 0) s1 = mfma32(FB[0], qf[0], negm); else s1 = mfma32(FB[I], qf[I], s1);
        SBAR();
        if constexpr (decltype(RDV)::value) FA[I] = vread(I, 0);
        if constexpr (I == 4) { mx = max3(s0[0], s0[1], s0[2]); mx = max3(mx, s0[3], s0[4]); PIN1(mx); }
        if constexpr (I == 5) { mx = max3(mx, s0[5], s0[6]); mx = max3(mx, s0[7], s0[8]); PIN1(mx); }
        if constexpr (I == 6) { mx = max3(mx, s0[9], s0[10]); mx = max3(mx, s0[11], s0[12]); PIN1(mx); }
        if constexpr (I == 7) { mx = max3(mx, s0[13], s0[14]); mx = max3(mx, s0[15], s0[15]); PIN1(mx); }
        SBAR();
      });
    };
    auto rescale_o = [&]() {
      if (__builtin_expect(pend, 0)) {
#pragma unroll
        for (int T = 0; T < 4; ++T) acc[T] *= al_pend;
        pend = false;
      }
    };
    auto H3 = [&]() {
      for_seq(std::make_integer_sequence<int, 4>{}, [&](auto IC_) {
        constexpr int I = decltype(IC_)::value;
        acc[I & 3] = mfma32(FA[I], asv(pf[I >> 2]), acc[I & 3]);
        SBAR();
        FB[I] = vread(I, 1);
        if constexpr (I == 2) {
          mx = max3(mx, s1[0], s1[1]); mx = max3(mx, s1[2], s1[3]); mx = max3(mx, s1[4], s1[5]); mx = max3(mx, s1[6], s1[7]); PIN1(mx);
        }
        if constexpr (I == 3) {
          mx = max3(mx, s1[8], s1[9]); mx = max3(mx, s1[10], s1[11]); mx = max3(mx, s1[12], s1[13]); mx = max3(mx, s1[14], s1[15]); PIN1(mx);
        }
        SBAR();
      });
      mx = xmax32(mx);
      if (__builtin_expect(__any(mx > THR), 0)) {
        float d = fmaxf(mx, 0.f);
        al_pend = __builtin_amdgcn_exp2f(-d);
        pend = true;
        l_run = (l_run + ps0 + ps1) * al_pend;
        ps0 = 0.f; ps1 = 0.f;
#pragma unroll
        for (int r = 0; r < 16; ++r) { negm[r] -= d; s0[r] -= d; s1[r] -= d; }
      }
      SBAR();
      for_seq(std::make_integer_sequence<int, 4>{}, [&](auto IC_) {
        constexpr int I = 4 + decltype(IC_)::value;
        acc[I & 3] = mfma32(FA[I], asv(pf[I >> 2]), acc[I & 3]);
        SBAR();
        FB[I] = vread(I, 1);
        if constexpr (I > 4) ADD2(s0, 2 * (I - 5));
        EXP2(s0, 2 * (I - 4));
        SBAR();
      });
    };
    auto H4 = [&](auto NEXTK) {
      for_seq(std::make_integer_sequence<int, 8>{}, [&](auto IC_) {
        constexpr int I = decltype(IC_)::value;
        acc[I & 3] = mfma32(FB[I], asv(pf[2 + (I >> 2)]), acc[I & 3]);
        SBAR();
        if constexpr (decltype(NEXTK)::value) FA[I] = kread(I, 0);
        if constexpr (I < 5) ADD2(s0, 6 + 2 * I);
        if constexpr (I < 4) EXP2(s0, 8 + 2 * I);
        if constexpr (I >= 4) CVT2(s0, 2 * (I - 4), pf[0], pf[1]);
        if constexpr (I == 5) EXP2(s1, 0);
        if constexpr (I == 6) EXP2(s1, 2);
        if constexpr (I == 7) ADD2(s1, 0);
        SBAR();
      });
    };
    auto adv = [&](unsigned& a, int& slot) {
      slot += 2;
      unsigned d = 2 * SLOT;
      if (slot >= 9) { slot -= 9; d = (unsigned)(-7 * SLOT); }
      a += d;
    };

#pragma unroll
    for (int r = 0; r < 16; ++r) negm[r] = 0.f;
    barrier();
    STAMP(3);
#pragma unroll
    for (int F = 0; F < 8; ++F) FA[F] = kread(F, 0);
    SBAR();
    H1(F_{});
    H2(F_{});
#pragma unroll
    for (int i = 0; i < 8; ++i) mx = max3(mx, s1[2 * i], s1[2 * i + 1]);
    mx = xmax32(mx);
#pragma unroll
    for (int r = 0; r < 16; ++r) { negm[r] = -mx; s0[r] -= mx; s1[r] -= mx; }
    int kslot = 1, vslot = 2, kslot2 = 1;
    kad[0] += SLOT; kad[1] += SLOT; vad += 2 * SLOT;
    barrier_nw();
#pragma unroll
    for (int F = 0; F < 8; ++F) FA[F] = kread(F, 0);
#pragma unroll
    for (int r = 0; r < 16; r += 2) EXP2(s0, r);
#pragma unroll
    for (int r = 0; r < 16; r += 2) ADD2(s0, r);
#pragma unroll
    for (int i = 0; i < 8; i += 2) CVT2(s0, i, pf[0], pf[1]);
    EXP2(s1, 0); EXP2(s1, 2); ADD2(s1, 0);
    SBAR();
    H1(T_{});
#pragma unroll 1
    for (int j = 0; j < 6; ++j) {
      barrier_nw();
      SBAR();
      H2(T_{});
      H3();
      adv(kad[0], kslot); adv(kad[1], kslot2); adv(vad, vslot);
      SBAR();
      H4(T_{});
      H1(T_{});
      rescale_o();
    }
    barrier_nw();
    SBAR();
    H2(T_{});
    H3();
    H4(F_{});
    for_seq(std::make_integer_sequence<int, 8>{}, fin1);
    rescale_o();
    const float inv = 1.0f / xadd32(l_run + ps0 + ps1);
    vad += SLOT;
    char* op = ows + ((size_t)(b * 9 + 2 * wid + (r32 >> 4)) * 128 + h * 4) * 1024 + (hh * 32 + (r32 & 15)) * 16;
    STAMP(5);
    barrier_nw();
    STAMP(6);
    SBAR();
    {
      auto vr = [&](int T, int u) {
        const unsigned o = (4 * u) * GRP + 64 * T;
        return cat(trread(vad + o), trread(vad + o + 512));
      };
#pragma unroll
      for (int F = 0; F < 8; ++F) FA[F] = vr(F >> 2, F & 3);
#pragma unroll
      for (int F = 0; F < 8; ++F) FB[F] = vr(2 + (F >> 2), F & 3);
      SBAR();
      auto store_tile = [&](int T) {
#pragma unroll
        for (int s = 0; s < 2; ++s) {
          u32x4 w = {pk2(acc[T][8 * s + 0] * inv, acc[T][8 * s + 1] * inv), pk2(acc[T][8 * s + 2] * inv, acc[T][8 * s + 3] * inv),
                     pk2(acc[T][8 * s + 4] * inv, acc[T][8 * s + 5] * inv), pk2(acc[T][8 * s + 6] * inv, acc[T][8 * s + 7] * inv)};
          OSTORE((u32x4*)(op + T * 1024 + s * 256), w);
        }
      };
#pragma unroll
      for (int u = 0; u < 4; ++u) acc[0] = mfma32(FA[u], asv(pf[u]), acc[0]);
#pragma unroll
      for (int u = 0; u < 4; ++u) acc[1] = mfma32(FA[4 + u], asv(pf[u]), acc[1]);
      SBAR();
#pragma unroll
      for (int u = 0; u < 4; ++u) acc[2] = mfma32(FB[u], asv(pf[u]), acc[2]);
      store_tile(0);
      SBAR();
#pragma unroll
      for (int u = 0; u < 4; ++u) acc[3] = mfma32(FB[4 + u], asv(pf[u]), acc[3]);
      store_tile(1);
      SBAR();
      barrier_nw();
      store_tile(2);
      store_tile(3);
    }
  }
}

constexpr int K2_STAGE = 7168;
constexpr int K2_WAVE = 2 * K2_STAGE;
constexpr int K2_LDS = 8 * K2_WAVE;
__global__ __launch_bounds__(512) void k2_proj(const char* __restrict__ ows, const char* __restrict__ wo, char* __restrict__ out) {
  __shared__ __attribute__((aligned(256))) char smem[K2_LDS];
  const int lane = threadIdx.x & 63;
  const int wid = __builtin_amdgcn_readfirstlane(threadIdx.x >> 6);
  const int xcd = blockIdx.x & 7, idx = blockIdx.x >> 3;
  const int ig = 2 * (xcd & 1) + ((xcd >> 1) & 1), jg = xcd >> 2;
  const int nn = jg ? 4 : 5;
  if (idx >= 6 * nn) return;
  const int mtile = 6 * ig + idx / nn, ntile = (jg ? 5 : 0) + idx % nn;
  const unsigned lds0 = (unsigned)(size_t)(LDSP char*)smem;
  const unsigned lw = lds0 + wid * K2_WAVE;
  const char* ag = ows + ((size_t)(3 * mtile) * 128 + wid) * 1024;
  const char* bg = wo + (size_t)(32 * wid) * (DM * 2) + ntile * 128;
  const unsigned aoff = lane * 16;
  unsigned boff[4];
#pragma unroll
  for (int n = 0; n < 4; ++n) {
    const int row = 8 * n + (lane >> 3), f = ((row >> 1) & 1) | (((row >> 4) & 1) << 1);
    boff[n] = row * (DM * 2) + (((lane & 7) ^ (f << 1)) << 4);
  }
  auto issue = [&](int i, auto BC) {
    constexpr int Bf = decltype(BC)::value * K2_STAGE;
    const char* a = ag + (size_t)i * 8192;
    const char* bp = bg + (size_t)i * (256 * DM * 2);
    dma16i<K2A_NT, Bf>(a, aoff, lw);
    dma16i<K2A_NT, Bf + 1024>(a + 131072, aoff, lw);
    dma16i<K2A_NT, Bf + 2048>(a + 262144, aoff, lw);
    dma16i<K2B_NT, Bf + 3072>(bp, boff[0], lw);
    dma16i<K2B_NT, Bf + 4096>(bp, boff[1], lw);
    dma16i<K2B_NT, Bf + 5120>(bp, boff[2], lw);
    dma16i<K2B_NT, Bf + 6144>(bp, boff[3], lw);
  };
  issue(0, IC(0));
  issue(1, IC(1));
  const int g = lane >> 4, qp = (lane >> 2) & 3, p = lane & 3;
  unsigned bad[4];
  {
    const int f = ((qp >> 1) & 1) | ((g & 1) << 1);
#pragma unroll
    for (int t = 0; t < 4; ++t) bad[t] = lw + 3072 + (16 * (g & 1) + 4 * (g >> 1) + qp) * 128 + 32 * (t ^ f) + 8 * p;
  }
  const unsigned aad = lw + lane * 16;
  f32x4 acc[3][4];
#pragma unroll
  for (int m = 0; m < 3; ++m)
#pragma unroll
    for (int t = 0; t < 4; ++t) acc[m][t] = f32x4{0.f, 0.f, 0.f, 0.f};
  auto step = [&](int i, auto BC, auto LASTC) {
    constexpr int Bf = decltype(BC)::value * K2_STAGE;
    constexpr int MODE = decltype(LASTC)::value;
    if constexpr (MODE == 2) wait_vm<0>(); else wait_vm<7>();
    s16x8 af[3], bf[4];
#pragma unroll
    for (int m = 0; m < 3; ++m) af[m] = ldsread16(aad + Bf + m * 1024);
#pragma unroll
    for (int t = 0; t < 4; ++t) bf[t] = cat(trread(bad[t] + Bf), trread(bad[t] + Bf + 1024));
    asm volatile("s_waitcnt lgkmcnt(0)" ::: "memory");
    SBAR();
    if constexpr (MODE == 0) issue(i + 2, BC);
    SBAR();
#pragma unroll
    for (int m = 0; m < 3; ++m)
#pragma unroll
      for (int t = 0; t < 4; ++t) acc[m][t] = mfma16(bf[t], af[m], acc[m][t]);
    SBAR();
  };
#pragma unroll 1
  for (int i = 0; i < 14; i += 2) {
    step(i, IC(0), IC(0));
    step(i + 1, IC(1), IC(0));
  }
  step(14, IC(0), IC(1));
  step(15, IC(1), IC(2));
  barrier();
#pragma unroll
  for (int m = 0; m < 3; ++m)
#pragma unroll
    for (int t = 0; t < 4; ++t) *(LDSP f32x4*)(size_t)(lds0 + (wid * 12 + m * 4 + t) * 1024 + lane * 16) = acc[m][t];
  barrier();
#pragma unroll
  for (int rep = 0; rep < 2; ++rep) {
    const int u = rep * 8 + wid;
    if (u < 12) {
      f32x4 sum = *(LDSP f32x4*)(size_t)(lds0 + u * 1024 + lane * 16);
#pragma unroll
      for (int w = 1; w < 8; ++w) sum += *(LDSP f32x4*)(size_t)(lds0 + (w * 12 + u) * 1024 + lane * 16);
      const int m = u >> 2, t = u & 3;
      u32x2 v = {pk2(sum[0], sum[1]), pk2(sum[2], sum[3])};
      *(u32x2*)(out + ((size_t)(mtile * 48 + m * 16 + (lane & 15)) * DM + ntile * 64 + t * 16 + 4 * g) * 2) = v;
    }
  }
}

}

extern "C" void kernel_launch(void* const* d_in, const int* in_sizes, int n_in, void* d_out, int out_size, void* d_ws, size_t ws_size,
                              hipStream_t stream) {
  const char* x = (const char*)d_in[0];
  const char* kc = (const char*)d_in[1];
  const char* vc = (const char*)d_in[2];
  const char* wq = (const char*)d_in[3];
  const char* wo = (const char*)d_in[4];
  char* ows = (char*)d_ws;
  k1_attn<<<256, 512, 0, stream>>>(x, kc, vc, wq, wo, ows);
  k2_proj<<<256, 512, 0, stream>>>(ows, wo, (char*)d_out);
}
```

```cpp
#include <hip/hip_runtime.h>
#include <hip/hip_bf16.h>
#include <utility>

namespace {

constexpr int NB = 8, SQ = 144, DM = 576, NH = 32, SK = 512, DH = 128;

typedef __attribute__((ext_vector_type(4))) short s16x4;
typedef __attribute__((ext_vector_type(8))) short s16x8;
typedef __attribute__((ext_vector_type(4))) float f32x4;
typedef __attribute__((ext_vector_type(16))) float f32x16;
typedef __attribute__((ext_vector_type(8))) __bf16 bf16x8;
typedef __attribute__((ext_vector_type(2))) __bf16 bf16x2;
typedef __attribute__((ext_vector_type(2))) float f32x2;
typedef __attribute__((ext_vector_type(4))) unsigned u32x4;
typedef __attribute__((ext_vector_type(2))) unsigned u32x2;
#define LDSP __attribute__((address_space(3)))
#define DI __device__ __forceinline__

DI f32x4 mfma16(s16x8 a, s16x8 b, f32x4 c) {
  return __builtin_amdgcn_mfma_f32_16x16x32_bf16(__builtin_bit_cast(bf16x8, a), __builtin_bit_cast(bf16x8, b), c, 0, 0, 0);
}
DI f32x4 mfma16k16(s16x4 a, s16x4 b, f32x4 c) { return __builtin_amdgcn_mfma_f32_16x16x16bf16_1k(a, b, c, 0, 0, 0); }
DI f32x16 mfma32(s16x8 a, s16x8 b, f32x16 c) {
  return __builtin_amdgcn_mfma_f32_32x32x16_bf16(__builtin_bit_cast(bf16x8, a), __builtin_bit_cast(bf16x8, b), c, 0, 0, 0);
}
DI unsigned pk2(float lo, float hi) {
  f32x2 v = {lo, hi};
  bf16x2 r = __builtin_convertvector(v, bf16x2);
  return __builtin_bit_cast(unsigned, r);
}
DI s16x4 trread(unsigned addr) { return __builtin_amdgcn_ds_read_tr16_b64_v4i16((LDSP s16x4*)(size_t)addr); }
DI s16x8 ldsread16(unsigned addr) { return *(LDSP s16x8*)(size_t)addr; }
DI s16x8 cat(s16x4 a, s16x4 b) { return __builtin_shufflevector(a, b, 0, 1, 2, 3, 4, 5, 6, 7); }
DI float max3(float a, float b, float c) { return __builtin_fmaxf(__builtin_fmaxf(a, b), c); }
DI float xmax32(float v) {
  auto r = __builtin_amdgcn_permlane32_swap(__float_as_uint(v), __float_as_uint(v), false, false);
  return fmaxf(__uint_as_float(r[0]), __uint_as_float(r[1]));
}
DI float xmax16(float v) {
  auto r = __builtin_amdgcn_permlane16_swap(__float_as_uint(v), __float_as_uint(v), false, false);
  return fmaxf(__uint_as_float(r[0]), __uint_as_float(r[1]));
}
DI float xadd32(float v) {
  auto r = __builtin_amdgcn_permlane32_swap(__float_as_uint(v), __float_as_uint(v), false, false);
  return __uint_as_float(r[0]) + __uint_as_float(r[1]);
}
DI float xadd16(float v) {
  auto r = __builtin_amdgcn_permlane16_swap(__float_as_uint(v), __float_as_uint(v), false, false);
  return __uint_as_float(r[0]) + __uint_as_float(r[1]);
}

template <bool NT, int IMM>
DI void dma16i(const char* sbase, unsigned voff, unsigned lds) {
  if constexpr (NT)
    asm volatile("s_add_u32 m0, %1, %3\n\ts_nop 0\n\tglobal_load_lds_dwordx4 %0, %2 nt" ::"v"(voff), "s"(lds), "s"(sbase), "i"(IMM) : "memory", "scc");
  else
    asm volatile("s_add_u32 m0, %1, %3\n\ts_nop 0\n\tglobal_load_lds_dwordx4 %0, %2" ::"v"(voff), "s"(lds), "s"(sbase), "i"(IMM) : "memory", "scc");
}
template <int IMM>
DI void dma16i_lo(const char* sbase, unsigned voff, unsigned lds) {
  asm volatile("s_add_u32 m0, %1, %3\n\ts_mov_b32 exec_hi, 0\n\tglobal_load_lds_dwordx4 %0, %2\n\ts_mov_b32 exec_hi, -1" ::"v"(voff), "s"(lds), "s"(sbase), "i"(IMM) : "memory", "scc");
}
template <int N> DI void wait_vm() { asm volatile("s_waitcnt vmcnt(%0)" ::"i"(N) : "memory"); }
DI void barrier() { asm volatile("s_waitcnt lgkmcnt(0)\n\ts_barrier" ::: "memory"); }
DI void barrier_nw() { asm volatile("s_barrier" ::: "memory"); }
template <int... I, class F> DI void for_seq(std::integer_sequence<int, I...>, F&& f) { (f(std::integral_constant<int, I>{}), ...); }
#define IC(n) std::integral_constant<int, n>{}
#define SBAR() __builtin_amdgcn_sched_barrier(0)

constexpr bool KV_NT = true;
constexpr bool Q_NT = false, K2A_NT = false, K2B_NT = false;
constexpr int HPRIO = 2;
DI void OSTORE(u32x4* p, u32x4 v) { asm volatile("global_store_dwordx4 %0, %1, off sc1\n\ts_nop 1" ::"v"(p), "v"(v) : "memory"); }

constexpr int SLOT = 17408;
constexpr int GRP = 1088;
constexpr int QRING = 3;
constexpr int QPOS0 = 3 * SLOT;
constexpr int QX_OFF = 9 * SLOT;
constexpr int ST_OFF = QX_OFF + 4096;
constexpr int AUXF = ST_OFF + 512;
constexpr int DUMMY_OFF = AUXF + 128;
constexpr int K1_LDS = DUMMY_OFF + 1024;
constexpr int CMB_OFF = 7 * SLOT;
constexpr float CSC = 0.08838834764831845f * 1.4426950408889634f;
constexpr float THR = 8.0f;

constexpr int NEV = 19;
constexpr int need_ev(int it) {
  if (it < 9) return it;
  int a = it - 9;
  return a < 2 ? 9 + a : (a == 15 ? 18 : 11 + (a - 2) / 2);
}
constexpr int free_ev(int it) { return it < 9 ? it + 1 : need_ev(it) + 2; }
struct Sched { int n; int item[48]; int when[48]; };
constexpr int WOPF_EV = 16;
constexpr int LA2 = 4;
constexpr Sched make_sched() {
  Sched S{};
  int n = 0;
  auto add = [&](int it, int w) { S.item[n] = it; S.when[n] = w; ++n; };
  add(0, -1); add(1, -1);
  int nt = 0;
  for (int e = 0; e < NEV; ++e) {
    if (e <= 6) add(e + 2, e);
    int lim = (e == 7) ? 2 : (e == 8 ? 4 : (e == 9 ? LA2 + 1 : (e == 10 ? LA2 + 2 : (e >= 11 ? 2 * (e - 11) + 4 + LA2 : 0))));
    if (lim > 16) lim = 16;
    for (; nt < lim; ++nt) add(9 + nt, e);
    if (e == WOPF_EV) for (int k = 0; k < 5; ++k) add(100 + k, e);
  }
  for (int i = 1; i < n; ++i)
    for (int j = i; j > 0 && S.when[j - 1] > S.when[j]; --j) {
      int t = S.when[j]; S.when[j] = S.when[j - 1]; S.when[j - 1] = t;
      t = S.item[j]; S.item[j] = S.item[j - 1]; S.item[j - 1] = t;
    }
  S.n = n;
  return S;
}
constexpr Sched SCH = make_sched();
constexpr int item_ops(int it) { return it < 9 ? 9 : (it < 100 ? 4 : 1); }
constexpr int sched_wait(int ev) {
  int pos = -1, cnt = 0;
  for (int e = 0; e < SCH.n; ++e) if (SCH.item[e] < 100 && need_ev(SCH.item[e]) == ev) pos = e;
  for (int e = pos + 1; e < SCH.n; ++e) if (SCH.when[e] < ev) cnt += item_ops(SCH.item[e]);
  return cnt;
}
constexpr bool sched_ok() {
  for (int e = 0; e < SCH.n; ++e) {
    int it = SCH.item[e], w = SCH.when[e];
    if (it >= 100) continue;
    if (w >= need_ev(it)) return false;
    int prev = -1;
    if (it < 9) { if (it >= QRING) prev = it - QRING; }
    else { int a = it - 9; if (a >= 9) prev = it - 9; else if (a >= 3) prev = 6 + (a - 3) / 2; }
    if (prev >= 0 && w < free_ev(prev)) return false;
  }
  for (int ev = 0; ev < NEV; ++ev) if (sched_wait(ev) > 60) return false;
  return true;
}
static_assert(sched_ok(), "loader schedule violates a slot or ordering constraint");

__global__ __launch_bounds__(512) void k1_attn(const char* __restrict__ x, const char* __restrict__ kc, const char* __restrict__ vc,
                                               const char* __restrict__ wq, const char* __restrict__ wo, char* __restrict__ ows) {
  __shared__ __attribute__((aligned(256))) char smem[K1_LDS];
  const int tid = threadIdx.x;
  const int lane = tid & 63;
  const int wid = __builtin_amdgcn_readfirstlane(tid >> 6);
  const int xcd = blockIdx.x & 7, idx = blockIdx.x >> 3;
  const int b = 4 * (xcd & 1) + (idx & 3);
  const int h = 8 * (xcd >> 1) + (idx >> 2);
  const unsigned lds0 = (unsigned)(size_t)(LDSP char*)smem;

  if (wid >= 4) {
    const int hi = wid - 4;
    const char* xg = x + (size_t)(b * SQ) * (DM * 2);
    const char* wg = wq + h * 256;
    const char* kg = kc + (size_t)(b * NH + h) * (SK * DH * 2);
    const char* vg = vc + (size_t)(b * NH + h) * (SK * DH * 2);
    unsigned xoff[4], xoffe, woff[4], koff[4], voff[4];
    {
      const int cc = lane & 7, c16 = lane & 15;
#pragma unroll
      for (int n = 0; n < 4; ++n) {
        int rx = hi * 32 + n * 8 + (lane >> 3);
        xoff[n] = rx * (DM * 2) + ((cc ^ ((rx >> 1) & 7)) << 4);
        int r = hi * 16 + n * 4 + (lane >> 4);
        woff[n] = r * 8192 + ((c16 ^ (((r & 3) << 2) | (((r >> 3) & 1) << 1))) << 4);
        koff[n] = r * 256 + ((c16 ^ (r & 3)) << 4);
        voff[n] = r * 256 + (c16 << 4);
      }
      int rx = 128 + hi * 4 + ((lane >> 3) & 3);
      xoffe = rx * (DM * 2) + ((cc ^ ((rx >> 1) & 7)) << 4);
    }
    const unsigned lq = lds0 + QPOS0 + hi * 4096;
    const unsigned lqe = lds0 + QPOS0 + 32768 + hi * 512;
    const unsigned lt = lds0 + hi * (4 * GRP);
    const int qrot = (2 * (idx & 3) + (idx >> 2)) % 9;
    auto issue_q = [&](auto SC) {
      constexpr int S = decltype(SC)::value;
      constexpr int P = (S % 3) * (2 * SLOT);
      int sg = S + qrot; sg = sg >= 9 ? sg - 9 : sg;
      const char* ws = wg + (size_t)sg * (64 * 8192);
      const char* xs = xg + sg * 128;
      dma16i<Q_NT, P>(ws, woff[0], lq); dma16i<Q_NT, P + 1024>(ws, woff[1], lq);
      dma16i<Q_NT, P + 2048>(ws, woff[2], lq); dma16i<Q_NT, P + 3072>(ws, woff[3], lq);
      dma16i<Q_NT, P + 16384>(xs, xoff[0], lq); dma16i<Q_NT, P + 16384 + 1024>(xs, xoff[1], lq);
      dma16i<Q_NT, P + 16384 + 2048>(xs, xoff[2], lq); dma16i<Q_NT, P + 16384 + 3072>(xs, xoff[3], lq);
      dma16i_lo<P>(xs, xoffe, lqe);
    };
    auto issue_t = [&](auto AC) {
      constexpr int A = decltype(AC)::value;
      constexpr int L = (A % 9) * SLOT;
      constexpr bool isK = (A == 0) || ((A & 1) && A != 15);
      if constexpr (isK) {
        const char* sp = kg + ((A + 1) >> 1) * 16384;
        dma16i<KV_NT, L>(sp, koff[0], lt); dma16i<KV_NT, L + GRP>(sp, koff[1], lt);
        dma16i<KV_NT, L + 2 * GRP>(sp, koff[2], lt); dma16i<KV_NT, L + 3 * GRP>(sp, koff[3], lt);
      } else {
        const char* sp = vg + ((A == 15) ? 7 : ((A - 2) >> 1)) * 16384;
        dma16i<KV_NT, L>(sp, voff[0], lt); dma16i<KV_NT, L + GRP>(sp, voff[1], lt);
        dma16i<KV_NT, L + 2 * GRP>(sp, voff[2], lt); dma16i<KV_NT, L + 3 * GRP>(sp, voff[3], lt);
      }
    };
    const char* wop = wo + (size_t)blockIdx.x * 18432 + hi * 4608;
    auto issue_at = [&](auto CC) {
      constexpr int C = decltype(CC)::value;
      for_seq(std::make_integer_sequence<int, SCH.n>{}, [&](auto EC) {
        constexpr int E = decltype(EC)::value;
        if constexpr (SCH.when[E] == C) {
          constexpr int IT = SCH.item[E];
          if constexpr (IT < 9) issue_q(IC(IT));
          else if constexpr (IT < 100) issue_t(IC(IT - 9));
          else if constexpr (IT < 104) dma16i<false, DUMMY_OFF>(wop + (IT - 100) * 1024, lane * 16, lds0);
          else dma16i_lo<DUMMY_OFF>(wop + 4096, lane * 16, lds0);
        }
      });
    };
    issue_at(IC(-1));

    const int r16 = lane & 15, g = lane >> 4, qp = (lane >> 2) & 3, p = lane & 3;
    unsigned wbase[2], xbase[2], kb, vb;
    {
      const int ew = (qp << 1) | (g & 1);
#pragma unroll
      for (int t = 0; t < 2; ++t) wbase[t] = lds0 + QPOS0 + (8 * g + qp) * 256 + 32 * ((2 * hi + t) ^ ew) + 8 * p;
      const int rx = 128 + r16;
#pragma unroll
      for (int s = 0; s < 2; ++s) xbase[s] = lds0 + QPOS0 + 16384 + rx * 128 + (((4 * s + g) ^ ((rx >> 1) & 7)) << 4);
      kb = lds0 + (4 * hi + (r16 & 3)) * GRP + (r16 >> 2) * 256 + ((g ^ (r16 >> 2)) << 4);
      vb = lds0 + (4 * hi + qp) * GRP + g * 256 + 8 * p;
    }
    __builtin_amdgcn_s_setprio(HPRIO);
    if (lane == 0) *(volatile LDSP unsigned*)(size_t)(lds0 + AUXF + 4 * hi) = 0u;
    f32x4 qa[2] = {f32x4{0.f, 0.f, 0.f, 0.f}, f32x4{0.f, 0.f, 0.f, 0.f}};
    for_seq(std::make_integer_sequence<int, 9>{}, [&](auto SC) {
      constexpr int S = decltype(SC)::value;
      wait_vm<sched_wait(S)>();
      barrier();
      constexpr unsigned P = (S % 3) * (2 * SLOT);
      s16x8 xb[2], wf[2][2];
#pragma unroll
      for (int ks = 0; ks < 2; ++ks) {
        xb[ks] = ldsread16(xbase[ks] + P);
#pragma unroll
        for (int t = 0; t < 2; ++t) wf[ks][t] = cat(trread(wbase[t] + P + ks * 8192), trread(wbase[t] + P + ks * 8192 + 1024));
      }
      SBAR();
      issue_at(SC);
      SBAR();
#pragma unroll
      for (int ks = 0; ks < 2; ++ks)
#pragma unroll
        for (int t = 0; t < 2; ++t) {
          qa[t] = mfma16(wf[ks][t], xb[ks], qa[t]);
        }
    });
    {
      qa[0] *= CSC; qa[1] *= CSC;
      unsigned a0 = pk2(qa[0][0], qa[0][1]), a1 = pk2(qa[0][2], qa[0][3]);
      unsigned b0 = pk2(qa[1][0], qa[1][1]), b1 = pk2(qa[1][2], qa[1][3]);
      auto r0 = __builtin_amdgcn_permlane32_swap(a0, b0, false, false);
      auto r1 = __builtin_amdgcn_permlane32_swap(a1, b1, false, false);
      auto t0 = __builtin_amdgcn_permlane16_swap(r0[0], r0[1], false, false);
      auto t1 = __builtin_amdgcn_permlane16_swap(r1[0], r1[1], false, false);
      u32x4 w = {t0[0], t1[0], t0[1], t1[1]};
      *(LDSP u32x4*)(size_t)(lds0 + QX_OFF + hi * 1024 + lane * 16) = w;
    }
    s16x8 qf[4];
    float m_run = -__builtin_inff(), l_run = 0.f, al_pend = 1.f;
    f32x4 oa[8];
#pragma unroll
    for (int T = 0; T < 8; ++T) oa[T] = f32x4{0.f, 0.f, 0.f, 0.f};
    s16x4 p_cur = {0, 0, 0, 0}, p_nxt = {0, 0, 0, 0};
    bool hpend = false;
    auto kstep = [&](const s16x8 (&kf)[4], bool first) {
      f32x4 s = {0.f, 0.f, 0.f, 0.f};
#pragma unroll
      for (int ks = 0; ks < 4; ++ks) s = mfma16(kf[ks], qf[ks], s);
      float mx = max3(fmaxf(s[0], s[1]), s[2], s[3]);
      mx = xmax32(xmax16(mx));
      if (first) {
        m_run = mx;
      } else if (__builtin_expect(__any(mx > m_run + THR), 0)) {
        float m_new = fmaxf(m_run, mx);
        al_pend = __builtin_amdgcn_exp2f(m_run - m_new);
        m_run = m_new;
        l_run *= al_pend;
        hpend = true;
      }
#pragma unroll
      for (int r = 0; r < 4; ++r) { s[r] = __builtin_amdgcn_exp2f(s[r] - m_run); l_run += s[r]; }
      u32x2 w = {pk2(s[0], s[1]), pk2(s[2], s[3])};
      p_nxt = __builtin_bit_cast(s16x4, w);
      if (first) p_cur = p_nxt;
    };
    auto vstep = [&](const s16x4 (&vf)[8], bool last) {
#pragma unroll
      for (int T = 0; T < 8; ++T) oa[T] = mfma16k16(vf[T], p_cur, oa[T]);
      if (!last && __builtin_expect(hpend, 0)) {
#pragma unroll
        for (int T = 0; T < 8; ++T) oa[T] *= al_pend;
        hpend = false;
      }
      p_cur = p_nxt;
    };
    for_seq(std::make_integer_sequence<int, 10>{}, [&](auto EC) {
      constexpr int EV = 9 + decltype(EC)::value;
      wait_vm<sched_wait(EV)>();
      barrier();
      constexpr int AV = (EV >= 11) ? (EV == 18 ? 15 : 2 * (EV - 11) + 2) : -1;
      constexpr int AK = (EV == 9) ? 0 : (EV == 10 ? 1 : (EV <= 16 ? 2 * (EV - 11) + 3 : -1));
      if constexpr (EV == 9) {
#pragma unroll
        for (int s = 0; s < 4; ++s) qf[s] = ldsread16(lds0 + QX_OFF + s * 1024 + lane * 16);
      }
      s16x8 kf[4]; s16x4 vf[8];
      if constexpr (AV >= 0) {
#pragma unroll
        for (int T = 0; T < 8; ++T) vf[T] = trread(vb + (AV % 9) * SLOT + T * 32);
      }
      if constexpr (AK >= 0) {
#pragma unroll
        for (int ks = 0; ks < 4; ++ks) kf[ks] = ldsread16(kb + (AK % 9) * SLOT + ks * 64);
      }
      SBAR();
      issue_at(IC(EV));
      SBAR();
      if constexpr (AV >= 0) vstep(vf, AV == 15);
      if constexpr (AK >= 0) kstep(kf, AK == 0);
    });
    {
      float l = xadd32(xadd16(l_run));
      if (g == 0) {
        f32x2 st = {m_run, l};
        *(LDSP f32x2*)(size_t)(lds0 + ST_OFF + (hi * 16 + r16) * 8) = st;
      }
#pragma unroll
      for (int T = 0; T < 8; ++T) *(LDSP f32x4*)(size_t)(lds0 + CMB_OFF + (hi * 8 + T) * 1024 + lane * 16) = oa[T];
    }
    asm volatile("s_waitcnt lgkmcnt(0)" ::: "memory");
    if (lane == 0) *(volatile LDSP unsigned*)(size_t)(lds0 + AUXF + 4 * hi) = 1u;
    for (;;) {
      u32x4 f = *(volatile LDSP u32x4*)(size_t)(lds0 + AUXF);
      if (__builtin_amdgcn_readfirstlane(f[0] & f[1] & f[2] & f[3]) != 0) break;
      __builtin_amdgcn_s_sleep(1);
    }
    asm volatile("" ::: "memory");
    {
      f32x2 st[4];
#pragma unroll
      for (int k = 0; k < 4; ++k) st[k] = *(LDSP f32x2*)(size_t)(lds0 + ST_OFF + (k * 16 + r16) * 8);
      float m = fmaxf(fmaxf(st[0][0], st[1][0]), fmaxf(st[2][0], st[3][0]));
      float sc[4], l = 0.f;
#pragma unroll
      for (int k = 0; k < 4; ++k) { sc[k] = __builtin_amdgcn_exp2f(st[k][0] - m); l += sc[k] * st[k][1]; }
      float inv = 1.0f / l;
      const int T = 2 * hi + (g & 1);
      f32x4 lo = {0.f, 0.f, 0.f, 0.f}, hi4 = {0.f, 0.f, 0.f, 0.f};
#pragma unroll
      for (int k = 0; k < 4; ++k) {
        unsigned base = lds0 + CMB_OFF + (k * 8 + T) * 1024 + r16 * 16;
        f32x4 a = *(LDSP f32x4*)(size_t)(base + (g >> 1) * 256);
        f32x4 c2 = *(LDSP f32x4*)(size_t)(base + (2 + (g >> 1)) * 256);
        lo += a * sc[k];
        hi4 += c2 * sc[k];
      }
      u32x4 w = {pk2(lo[0] * inv, lo[1] * inv), pk2(lo[2] * inv, lo[3] * inv), pk2(hi4[0] * inv, hi4[1] * inv), pk2(hi4[2] * inv, hi4[3] * inv)};
      OSTORE((u32x4*)(ows + ((size_t)(b * 9 + 8) * 128 + h * 4 + hi) * 1024 + lane * 16), w);
    }
    wait_vm<0>();
  } else {
    const int r32 = lane & 31, hh = lane >> 5, cb = (lane >> 4) & 1, qp = (lane >> 2) & 3, p = lane & 3;
    f32x16 acc[4];
#pragma unroll
    for (int T = 0; T < 4; ++T)
#pragma unroll
      for (int r = 0; r < 16; ++r) acc[T][r] = 0.f;
    s16x8 FA[8], FB[8];
    using T_ = std::true_type; using F_ = std::false_type;

    {
      unsigned wad[4], xad[4];
#pragma unroll
      for (int T = 0; T < 4; ++T) {
        unsigned ch = ((4 * T + 2 * cb + (p >> 1)) ^ ((qp << 2) | (hh << 1)));
        wad[T] = lds0 + QPOS0 + (8 * hh + qp) * 256 + ch * 16 + 8 * (p & 1);
      }
      const int rx = wid * 32 + r32;
#pragma unroll
      for (int ks = 0; ks < 4; ++ks) xad[ks] = lds0 + QPOS0 + 16384 + rx * 128 + (((2 * ks + hh) ^ ((rx >> 1) & 7)) << 4);
      s16x8 xa[2], xb[2];
      auto qhalf = [&](s16x8 (&CUR)[8], s16x8 (&xc)[2], s16x8 (&NXT)[8], s16x8 (&xn)[2], auto HC, auto DOMM, auto DOLD) {
        constexpr int HALF = decltype(HC)::value;
        auto rdw = [&](int F) {
          const int k = F >> 2, T = F & 3;
          NXT[F] = cat(trread(wad[T] + (2 * HALF + k) * 4096), trread(wad[T] + (2 * HALF + k) * 4096 + 1024));
        };
        for_seq(std::make_integer_sequence<int, 8>{}, [&](auto IC_) {
          constexpr int I = decltype(IC_)::value;
          if constexpr (decltype(DOMM)::value) { acc[I & 3] = mfma32(CUR[I], xc[I >> 2], acc[I & 3]); SBAR(); }
          if constexpr (decltype(DOLD)::value) {
            if constexpr (I == 0) { xn[0] = ldsread16(xad[2 * HALF]); rdw(0); }
            if constexpr (I == 1) { rdw(1); rdw(2); }
            if constexpr (I == 2) { rdw(3); xn[1] = ldsread16(xad[2 * HALF + 1]); }
            if constexpr (I == 3) { rdw(4); rdw(5); }
            if constexpr (I == 4) { rdw(6); }
            if constexpr (I == 5) { rdw(7); }
          }
          SBAR();
        });
      };
      barrier();
      qhalf(FB, xb, FA, xa, IC(0), F_{}, T_{});
      qhalf(FA, xa, FB, xb, IC(1), T_{}, T_{});
      int pos = 0;
#pragma unroll 1
      for (int S = 0; S < 8; ++S) {
        const unsigned d = (pos == QRING - 1) ? (unsigned)(-2 * (QRING - 1) * SLOT) : (unsigned)(2 * SLOT);
        pos = (pos == QRING - 1) ? 0 : pos + 1;
#pragma unroll
        for (int T = 0; T < 4; ++T) { wad[T] += d; xad[T] += d; }
        asm volatile("" : "+v"(wad[0]), "+v"(wad[1]), "+v"(wad[2]), "+v"(wad[3]), "+v"(xad[0]), "+v"(xad[1]), "+v"(xad[2]), "+v"(xad[3]));
        barrier();
        SBAR();
        qhalf(FB, xb, FA, xa, IC(0), T_{}, T_{});
        qhalf(FA, xa, FB, xb, IC(1), T_{}, T_{});
      }
      qhalf(FB, xb, FA, xa, IC(0), T_{}, F_{});
    }

    s16x8 qf[8];
#pragma unroll
    for (int T = 0; T < 4; ++T) {
      acc[T] *= CSC;
#pragma unroll
      for (int s = 0; s < 2; ++s) {
        unsigned a0 = pk2(acc[T][8 * s + 0], acc[T][8 * s + 1]), a1 = pk2(acc[T][8 * s + 2], acc[T][8 * s + 3]);
        unsigned b0 = pk2(acc[T][8 * s + 4], acc[T][8 * s + 5]), b1 = pk2(acc[T][8 * s + 6], acc[T][8 * s + 7]);
        auto r0 = __builtin_amdgcn_permlane32_swap(a0, b0, false, false);
        auto r1 = __builtin_amdgcn_permlane32_swap(a1, b1, false, false);
        u32x4 w = {r0[0], r1[0], r0[1], r1[1]};
        qf[2 * T + s] = __builtin_bit_cast(s16x8, w);
      }
    }
#pragma unroll
    for (int T = 0; T < 4; ++T)
#pragma unroll
      for (int r = 0; r < 16; ++r) acc[T][r] = 0.f;

    unsigned kad[2], vad;
    {
      const int a_ = r32 >> 3, h_ = (r32 >> 2) & 1, j_ = r32 & 3;
      const int grp = 4 * (a_ >> 1) + j_, rin = 2 * (a_ & 1) + h_;
#pragma unroll
      for (int e = 0; e < 2; ++e) kad[e] = lds0 + grp * GRP + rin * 256 + (((2 * e + hh) ^ rin) << 4);
      vad = lds0 + qp * GRP + hh * 256 + 32 * cb + 8 * p;
    }
    f32x16 s0, s1, negm;
    u32x4 pf[4];
    float l_run = 0.f, al_pend = 1.f, mx = 0.f, ps0 = 0.f, ps1 = 0.f;
    bool pend = false;
#define PIN1(a) asm volatile("" : "+v"(a))
#define PIN2(a, b) asm volatile("" : "+v"(a), "+v"(b))
    auto kread = [&](int F, int t) { return ldsread16(kad[F & 1] + t * (8 * GRP) + (F >> 1) * 64); };
    auto vread = [&](int F, int hf) {
      const unsigned o = (8 * hf + 4 * (F >> 2)) * GRP + 64 * (F & 3);
      return cat(trread(vad + o), trread(vad + o + 512));
    };
    auto EXP2 = [&](f32x16& s, int r) {
      float e0 = __builtin_amdgcn_exp2f(s[r]), e1 = __builtin_amdgcn_exp2f(s[r + 1]);
      PIN2(e0, e1);
      s[r] = e0; s[r + 1] = e1;
    };
    auto ADD2 = [&](const f32x16& s, int q) {
      ps0 += s[q]; ps1 += s[q + 1]; PIN2(ps0, ps1); };
    auto CVT2 = [&](const f32x16& s, int i, u32x4& lo, u32x4& hi2) {
      unsigned w0 = pk2(s[2 * i], s[2 * i + 1]), w1 = pk2(s[2 * i + 2], s[2 * i + 3]);
      PIN2(w0, w1);
      if (i < 4) { lo[i] = w0; lo[i + 1] = w1; } else { hi2[i - 4] = w0; hi2[i - 3] = w1; }
    };
    auto asv = [](const u32x4& v) { return __builtin_bit_cast(s16x8, v); };
    auto fin1 = [&](auto IC_) {
      constexpr int I = decltype(IC_)::value;
      if constexpr (I < 7) ADD2(s1, 2 + 2 * I);
      if constexpr (I < 6) EXP2(s1, 4 + 2 * I);
      if constexpr (I == 6) { CVT2(s1, 0, pf[2], pf[3]); CVT2(s1, 2, pf[2], pf[3]); }
      if constexpr (I == 7) { CVT2(s1, 4, pf[2], pf[3]); CVT2(s1, 6, pf[2], pf[3]); }
    };

    auto H1 = [&](auto FIN) {
      for_seq(std::make_integer_sequence<int, 8>{}, [&](auto IC_) {
        constexpr int I = decltype(IC_)::value;
        if constexpr (I == 0) s0 = mfma32(FA[0], qf[0], negm); else s0 = mfma32(FA[I], qf[I], s0);
        SBAR();
        FB[I] = kread(I, 1);
        if constexpr (decltype(FIN)::value) fin1(IC_);
        SBAR();
      });
    };
    auto H2 = [&](auto RDV) {
      for_seq(std::make_integer_sequence<int, 8>{}, [&](auto IC_) {
        constexpr int I = decltype(IC_)::value;
        if constexpr (I == 0) s1 = mfma32(FB[0], qf[0], negm); else s1 = mfma32(FB[I], qf[I], s1);
        SBAR();
        if constexpr (decltype(RDV)::value) FA[I] = vread(I, 0);
        if constexpr (I == 4) { mx = max3(s0[0], s0[1], s0[2]); mx = max3(mx, s0[3], s0[4]); PIN1(mx); }
        if constexpr (I == 5) { mx = max3(mx, s0[5], s0[6]); mx = max3(mx, s0[7], s0[8]); PIN1(mx); }
        if constexpr (I == 6) { mx = max3(mx, s0[9], s0[10]); mx = max3(mx, s0[11], s0[12]); PIN1(mx); }
        if constexpr (I == 7) { mx = max3(mx, s0[13], s0[14]); mx = max3(mx, s0[15], s0[15]); PIN1(mx); }
        SBAR();
      });
    };
    auto rescale_o = [&]() {
      if (__builtin_expect(pend, 0)) {
#pragma unroll
        for (int T = 0; T < 4; ++T) acc[T] *= al_pend;
        pend = false;
      }
    };
    auto H3 = [&]() {
      for_seq(std::make_integer_sequence<int, 4>{}, [&](auto IC_) {
        constexpr int I = decltype(IC_)::value;
        acc[I & 3] = mfma32(FA[I], asv(pf[I >> 2]), acc[I & 3]);
        SBAR();
        FB[I] = vread(I, 1);
        if constexpr (I == 2) {
          mx = max3(mx, s1[0], s1[1]); mx = max3(mx, s1[2], s1[3]); mx = max3(mx, s1[4], s1[5]); mx = max3(mx, s1[6], s1[7]); PIN1(mx);
        }
        if constexpr (I == 3) {
          mx = max3(mx, s1[8], s1[9]); mx = max3(mx, s1[10], s1[11]); mx = max3(mx, s1[12], s1[13]); mx = max3(mx, s1[14], s1[15]); PIN1(mx);
        }
        SBAR();
      });
      mx = xmax32(mx);
      if (__builtin_expect(__any(mx > THR), 0)) {
        float d = fmaxf(mx, 0.f);
        al_pend = __builtin_amdgcn_exp2f(-d);
        pend = true;
        l_run = (l_run + ps0 + ps1) * al_pend;
        ps0 = 0.f; ps1 = 0.f;
#pragma unroll
        for (int r = 0; r < 16; ++r) { negm[r] -= d; s0[r] -= d; s1[r] -= d; }
      }
      SBAR();
      for_seq(std::make_integer_sequence<int, 4>{}, [&](auto IC_) {
        constexpr int I = 4 + decltype(IC_)::value;
        acc[I & 3] = mfma32(FA[I], asv(pf[I >> 2]), acc[I & 3]);
        SBAR();
        FB[I] = vread(I, 1);
        if constexpr (I > 4) ADD2(s0, 2 * (I - 5));
        EXP2(s0, 2 * (I - 4));
        SBAR();
      });
    };
    auto H4 = [&](auto NEXTK) {
      for_seq(std::make_integer_sequence<int, 8>{}, [&](auto IC_) {
        constexpr int I = decltype(IC_)::value;
        acc[I & 3] = mfma32(FB[I], asv(pf[2 + (I >> 2)]), acc[I & 3]);
        SBAR();
        if constexpr (decltype(NEXTK)::value) FA[I] = kread(I, 0);
        if constexpr (I < 5) ADD2(s0, 6 + 2 * I);
        if constexpr (I < 4) EXP2(s0, 8 + 2 * I);
        if constexpr (I >= 4) CVT2(s0, 2 * (I - 4), pf[0], pf[1]);
        if constexpr (I == 5) EXP2(s1, 0);
        if constexpr (I == 6) EXP2(s1, 2);
        if constexpr (I == 7) ADD2(s1, 0);
        SBAR();
      });
    };
    auto adv = [&](unsigned& a, int& slot) {
      slot += 2;
      unsigned d = 2 * SLOT;
      if (slot >= 9) { slot -= 9; d = (unsigned)(-7 * SLOT); }
      a += d;
    };

#pragma unroll
    for (int r = 0; r < 16; ++r) negm[r] = 0.f;
    barrier();
#pragma unroll
    for (int F = 0; F < 8; ++F) FA[F] = kread(F, 0);
    SBAR();
    H1(F_{});
    H2(F_{});
#pragma unroll
    for (int i = 0; i < 8; ++i) mx = max3(mx, s1[2 * i], s1[2 * i + 1]);
    mx = xmax32(mx);
#pragma unroll
    for (int r = 0; r < 16; ++r) { negm[r] = -mx; s0[r] -= mx; s1[r] -= mx; }
    int kslot = 1, vslot = 2, kslot2 = 1;
    kad[0] += SLOT; kad[1] += SLOT; vad += 2 * SLOT;
    barrier_nw();
#pragma unroll
    for (int F = 0; F < 8; ++F) FA[F] = kread(F, 0);
#pragma unroll
    for (int r = 0; r < 16; r += 2) EXP2(s0, r);
#pragma unroll
    for (int r = 0; r < 16; r += 2) ADD2(s0, r);
#pragma unroll
    for (int i = 0; i < 8; i += 2) CVT2(s0, i, pf[0], pf[1]);
    EXP2(s1, 0); EXP2(s1, 2); ADD2(s1, 0);
    SBAR();
    H1(T_{});
#pragma unroll 1
    for (int j = 0; j < 6; ++j) {
      barrier_nw();
      SBAR();
      H2(T_{});
      H3();
      adv(kad[0], kslot); adv(kad[1], kslot2); adv(vad, vslot);
      SBAR();
      H4(T_{});
      H1(T_{});
      rescale_o();
    }
    barrier_nw();
    SBAR();
    H2(T_{});
    H3();
    H4(F_{});
    for_seq(std::make_integer_sequence<int, 8>{}, fin1);
    rescale_o();
    const float inv = 1.0f / xadd32(l_run + ps0 + ps1);
    vad += SLOT;
    char* op = ows + ((size_t)(b * 9 + 2 * wid + (r32 >> 4)) * 128 + h * 4) * 1024 + (hh * 32 + (r32 & 15)) * 16;
    barrier_nw();
    SBAR();
    {
      auto vr = [&](int T, int u) {
        const unsigned o = (4 * u) * GRP + 64 * T;
        return cat(trread(vad + o), trread(vad + o + 512));
      };
#pragma unroll
      for (int F = 0; F < 8; ++F) FA[F] = vr(F >> 2, F & 3);
#pragma unroll
      for (int F = 0; F < 8; ++F) FB[F] = vr(2 + (F >> 2), F & 3);
      SBAR();
      auto store_tile = [&](int T) {
#pragma unroll
        for (int s = 0; s < 2; ++s) {
          u32x4 w = {pk2(acc[T][8 * s + 0] * inv, acc[T][8 * s + 1] * inv), pk2(acc[T][8 * s + 2] * inv, acc[T][8 * s + 3] * inv),
                     pk2(acc[T][8 * s + 4] * inv, acc[T][8 * s + 5] * inv), pk2(acc[T][8 * s + 6] * inv, acc[T][8 * s + 7] * inv)};
          OSTORE((u32x4*)(op + T * 1024 + s * 256), w);
        }
      };
#pragma unroll
      for (int u = 0; u < 4; ++u) acc[0] = mfma32(FA[u], asv(pf[u]), acc[0]);
#pragma unroll
      for (int u = 0; u < 4; ++u) acc[1] = mfma32(FA[4 + u], asv(pf[u]), acc[1]);
      SBAR();
#pragma unroll
      for (int u = 0; u < 4; ++u) acc[2] = mfma32(FB[u], asv(pf[u]), acc[2]);
      store_tile(0);
      SBAR();
#pragma unroll
      for (int u = 0; u < 4; ++u) acc[3] = mfma32(FB[4 + u], asv(pf[u]), acc[3]);
      store_tile(1);
      SBAR();
      store_tile(2);
      store_tile(3);
    }
  }
}

constexpr int K2_STAGE = 7168;
constexpr int K2_WAVE = 2 * K2_STAGE;
constexpr int K2_LDS = 8 * K2_WAVE;
__global__ __launch_bounds__(512) void k2_proj(const char* __restrict__ ows, const char* __restrict__ wo, char* __restrict__ out) {
  __shared__ __attribute__((aligned(256))) char smem[K2_LDS];
  const int lane = threadIdx.x & 63;
  const int wid = __builtin_amdgcn_readfirstlane(threadIdx.x >> 6);
  const int xcd = blockIdx.x & 7, idx = blockIdx.x >> 3;
  const int ig = 2 * (xcd & 1) + ((xcd >> 1) & 1), jg = xcd >> 2;
  const int nn = jg ? 4 : 5;
  if (idx >= 6 * nn) return;
  const int mtile = 6 * ig + idx / nn, ntile = (jg ? 5 : 0) + idx % nn;
  const unsigned lds0 = (unsigned)(size_t)(LDSP char*)smem;
  const unsigned lw = lds0 + wid * K2_WAVE;
  const char* ag = ows + ((size_t)(3 * mtile) * 128 + wid) * 1024;
  const char* bg = wo + (size_t)(32 * wid) * (DM * 2) + ntile * 128;
  const unsigned aoff = lane * 16;
  unsigned boff[4];
#pragma unroll
  for (int n = 0; n < 4; ++n) {
    const int row = 8 * n + (lane >> 3), f = ((row >> 1) & 1) | (((row >> 4) & 1) << 1);
    boff[n] = row * (DM * 2) + (((lane & 7) ^ (f << 1)) << 4);
  }
  auto issue = [&](int i, auto BC) {
    constexpr int Bf = decltype(BC)::value * K2_STAGE;
    const char* a = ag + (size_t)i * 8192;
    const char* bp = bg + (size_t)i * (256 * DM * 2);
    dma16i<K2A_NT, Bf>(a, aoff, lw);
    dma16i<K2A_NT, Bf + 1024>(a + 131072, aoff, lw);
    dma16i<K2A_NT, Bf + 2048>(a + 262144, aoff, lw);
    dma16i<K2B_NT, Bf + 3072>(bp, boff[0], lw);
    dma16i<K2B_NT, Bf + 4096>(bp, boff[1], lw);
    dma16i<K2B_NT, Bf + 5120>(bp, boff[2], lw);
    dma16i<K2B_NT, Bf + 6144>(bp, boff[3], lw);
  };
  issue(0, IC(0));
  issue(1, IC(1));
  const int g = lane >> 4, qp = (lane >> 2) & 3, p = lane & 3;
  unsigned bad[4];
  {
    const int f = ((qp >> 1) & 1) | ((g & 1) << 1);
#pragma unroll
    for (int t = 0; t < 4; ++t) bad[t] = lw + 3072 + (16 * (g & 1) + 4 * (g >> 1) + qp) * 128 + 32 * (t ^ f) + 8 * p;
  }
  const unsigned aad = lw + lane * 16;
  f32x4 acc[3][4];
#pragma unroll
  for (int m = 0; m < 3; ++m)
#pragma unroll
    for (int t = 0; t < 4; ++t) acc[m][t] = f32x4{0.f, 0.f, 0.f, 0.f};
  auto step = [&](int i, auto BC, auto LASTC) {
    constexpr int Bf = decltype(BC)::value * K2_STAGE;
    constexpr int MODE = decltype(LASTC)::value;
    if constexpr (MODE == 2) wait_vm<0>(); else wait_vm<7>();
    s16x8 af[3], bf[4];
#pragma unroll
    for (int m = 0; m < 3; ++m) af[m] = ldsread16(aad + Bf + m * 1024);
#pragma unroll
    for (int t = 0; t < 4; ++t) bf[t] = cat(trread(bad[t] + Bf), trread(bad[t] + Bf + 1024));
    asm volatile("s_waitcnt lgkmcnt(0)" ::: "memory");
    SBAR();
    if constexpr (MODE == 0) issue(i + 2, BC);
    SBAR();
#pragma unroll
    for (int m = 0; m < 3; ++m)
#pragma unroll
      for (int t = 0; t < 4; ++t) acc[m][t] = mfma16(bf[t], af[m], acc[m][t]);
    SBAR();
  };
#pragma unroll 1
  for (int i = 0; i < 14; i += 2) {
    step(i, IC(0), IC(0));
    step(i + 1, IC(1), IC(0));
  }
  step(14, IC(0), IC(1));
  step(15, IC(1), IC(2));
#pragma unroll
  for (int m = 0; m < 3; ++m)
#pragma unroll
    for (int t = 0; t < 4; ++t) *(LDSP f32x4*)(size_t)(lw + (m * 4 + t) * 1024 + lane * 16) = acc[m][t];
  barrier();
#pragma unroll
  for (int rep = 0; rep < 2; ++rep) {
    const int u = rep * 8 + wid;
    if (u < 12) {
      f32x4 sum = *(LDSP f32x4*)(size_t)(lds0 + u * 1024 + lane * 16);
#pragma unroll
      for (int w = 1; w < 8; ++w) sum += *(LDSP f32x4*)(size_t)(lds0 + w * K2_WAVE + u * 1024 + lane * 16);
      const int m = u >> 2, t = u & 3;
      u32x2 v = {pk2(sum[0], sum[1]), pk2(sum[2], sum[3])};
      *(u32x2*)(out + ((size_t)(mtile * 48 + m * 16 + (lane & 15)) * DM + ntile * 64 + t * 16 + 4 * g) * 2) = v;
    }
  }
}

}

extern "C" void kernel_launch(void* const* d_in, const int* in_sizes, int n_in, void* d_out, int out_size, void* d_ws, size_t ws_size,
                              hipStream_t stream) {
  const char* x = (const char*)d_in[0];
  const char* kc = (const char*)d_in[1];
  const char* vc = (const char*)d_in[2];
  const char* wq = (const char*)d_in[3];
  const char* wo = (const char*)d_in[4];
  char* ows = (char*)d_ws;
  k1_attn<<<256, 512, 0, stream>>>(x, kc, vc, wq, wo, ows);
  k2_proj<<<256, 512, 0, stream>>>(ows, wo, (char*)d_out);
}
```
